```python
import torch

def _softmax(x, axis=-1):
    return torch.softmax(x, dim=axis)
def _gelu(x):
    return torch.nn.functional.gelu(x, approximate="tanh")
def _silu(x):
    return torch.nn.functional.silu(x)
def _sigmoid(x):
    return torch.sigmoid(x)
def _layernorm(x, axis=-1, eps=1e-5):
    m = x.mean(dim=axis, keepdim=True)
    v = x.var(dim=axis, keepdim=True, unbiased=False)
    return (x - m) / torch.sqrt(v + eps)
def _scatter_add(tgt, idx, src, axis=0):
    return tgt.clone().index_add_(axis, idx, src)
def _segment_sum(src, seg, n):
    out = torch.zeros((n,) + src.shape[1:], dtype=src.dtype)
    return out.index_add_(0, seg, src)
def _pad(x, pw):
    flat = tuple(v for pair in reversed(pw) for v in pair)
    return torch.nn.functional.pad(x, flat)

kernel_name = 'concat_heads__gqa_block_cached__mha_block_cached__near_ridge'

def setup_inputs():

    _w = (lambda a: (lambda t: t.to(torch.bfloat16) if t.is_floating_point() else t)(torch.as_tensor(a)))
    def _A_setup():
        B, Sq, D = 8, 160, 512
        Hq, Hkv, Sk, Dh = 32, 32, 512, 128
        assert Hq % Hkv == 0, (Hq, Hkv)
        return {
            "x": _w((lambda *s: torch.randn(*s, dtype=torch.bfloat16) * 0.8)(B, Sq, D)),
            "k_cache": _w((lambda *s: torch.randn(*s, dtype=torch.bfloat16) * 0.8)(B, Hkv, Sk, Dh)),
            "v_cache": _w((lambda *s: torch.randn(*s, dtype=torch.bfloat16) * 0.05)(B, Hkv, Sk, Dh)),
            "wq": _w((lambda *s: torch.randn(*s, dtype=torch.bfloat16) * 0.8)(D, Hq * Dh) * (D ** -0.5)),
            "wo": _w((lambda *s: torch.randn(*s, dtype=torch.bfloat16) * 0.05)(Hq * Dh, D) * ((Hq * Dh) ** -0.5)),
        }
    _A_in = _A_setup()

    def _B_setup():
        B, Sq, D = 8, 160, 512
        H, Sk, Dh = 32, 512, 128
        return {
            "x": _w((lambda *s: torch.randn(*s, dtype=torch.bfloat16) * 0.8)(B, Sq, D)),
            "k_cache": _w((lambda *s: torch.randn(*s, dtype=torch.bfloat16) * 0.8)(B, H, Sk, Dh)),
            "v_cache": _w((lambda *s: torch.randn(*s, dtype=torch.bfloat16) * 0.05)(B, H, Sk, Dh)),
            "wq": _w((lambda *s: torch.randn(*s, dtype=torch.bfloat16) * 0.8)(D, H * Dh) * (D ** -0.5)),
            "wo": _w((lambda *s: torch.randn(*s, dtype=torch.bfloat16) * 0.05)(H * Dh, D) * ((H * Dh) ** -0.5)),
        }
    _B_in = _B_setup()

    return {
        "A_x": _A_in["x"],
        "A_k_cache": _A_in["k_cache"],
        "A_v_cache": _A_in["v_cache"],
        "A_wq": _A_in["wq"],
        "A_wo": _A_in["wo"],
        "B_x": _B_in["x"],
        "B_k_cache": _B_in["k_cache"],
        "B_v_cache": _B_in["v_cache"],
        "B_wq": _B_in["wq"],
        "B_wo": _B_in["wo"],
    }

def reference(A_x, A_k_cache, A_v_cache, A_wq, A_wo, B_x, B_k_cache, B_v_cache, B_wq, B_wo):
    def _A_ref(x, k_cache, v_cache, wq, wo):
        B, Sq, D = x.shape
        Hq, Dh = 32, 128
        Hkv = k_cache.shape[1]
        rep = Hq // Hkv
        q = (lambda x, *d: x.permute(*d))((x @ wq).reshape(B, Sq, Hq, Dh), 0, 2, 1, 3)
        k = (lambda x, n, axis: torch.repeat_interleave(x, n, dim=axis))(k_cache, rep, axis=1)
        v = (lambda x, n, axis: torch.repeat_interleave(x, n, dim=axis))(v_cache, rep, axis=1)
        scores = torch.einsum("bhqd,bhkd->bhqk", q, k) * (Dh ** -0.5)
        p = _softmax(scores, axis=-1)
        o = torch.einsum("bhqk,bhkd->bhqd", p, v)
        return (lambda x, *d: x.permute(*d))(o, 0, 2, 1, 3).reshape(B, Sq, Hq * Dh) @ wo

    def _B_ref(x, k_cache, v_cache, wq, wo):
        B, Sq, D = x.shape
        H, Dh = 32, 128
        q = (lambda x, *d: x.permute(*d))((x @ wq).reshape(B, Sq, H, Dh), 0, 2, 1, 3)
        scores = torch.einsum("bhqd,bhkd->bhqk", q, k_cache) * (Dh ** -0.5)
        p = _softmax(scores, axis=-1)
        o = torch.einsum("bhqk,bhkd->bhqd", p, v_cache)
        return (lambda x, *d: x.permute(*d))(o, 0, 2, 1, 3).reshape(B, Sq, H * Dh) @ wo

    a = _A_ref(A_x, A_k_cache, A_v_cache, A_wq, A_wo)
    b = _B_ref(B_x, B_k_cache, B_v_cache, B_wq, B_wo)
    return torch.concatenate([a, b], axis=-1)
```

```cpp
#include <hip/hip_runtime.h>
#include <hip/hip_bf16.h>
typedef unsigned short u16;
static __device__ __forceinline__ float bf2f(u16 v) { return __uint_as_float(((unsigned)v) << 16); }
static __device__ __forceinline__ u16 f2bf(float f) { __bf16 b = (__bf16)f; return __builtin_bit_cast(u16, b); }
constexpr int B = 8, SQ = 160, D = 512, H = 32, SK = 512, DH = 128;

__global__ void naive_gemm(const u16* __restrict__ A, const u16* __restrict__ W, u16* __restrict__ C, int M, int N, int K, int ldc) {
  int n = blockIdx.x * 256 + threadIdx.x;
  int m0 = blockIdx.y * 8;
  float acc[8] = {0, 0, 0, 0, 0, 0, 0, 0};
  for (int k = 0; k < K; ++k) {
    float w = bf2f(W[(size_t)k * N + n]);
#pragma unroll
    for (int i = 0; i < 8; ++i) acc[i] += bf2f(A[(size_t)(m0 + i) * K + k]) * w;
  }
#pragma unroll
  for (int i = 0; i < 8; ++i) C[(size_t)(m0 + i) * ldc + n] = f2bf(acc[i]);
}

__global__ void naive_attn(const u16* __restrict__ q, const u16* __restrict__ k, const u16* __restrict__ v, u16* __restrict__ o) {
  __shared__ float s[SK];
  __shared__ float qs[DH];
  __shared__ float red[256];
  int row = blockIdx.x;
  int h = blockIdx.y;
  int b = row / SQ;
  int t = threadIdx.x;
  if (t < DH) qs[t] = bf2f(q[(size_t)row * (H * DH) + h * DH + t]);
  __syncthreads();
  const u16* kb = k + (size_t)(b * H + h) * SK * DH;
  const u16* vb = v + (size_t)(b * H + h) * SK * DH;
  float mx = -1e30f;
  for (int j = t; j < SK; j += 256) {
    float a = 0;
    for (int d = 0; d < DH; ++d) a += qs[d] * bf2f(kb[(size_t)j * DH + d]);
    a *= 0.08838834764831845f;
    s[j] = a;
    mx = fmaxf(mx, a);
  }
  red[t] = mx;
  __syncthreads();
  for (int st = 128; st > 0; st >>= 1) { if (t < st) red[t] = fmaxf(red[t], red[t + st]); __syncthreads(); }
  mx = red[0];
  __syncthreads();
  float sm = 0;
  for (int j = t; j < SK; j += 256) { float e = __expf(s[j] - mx); s[j] = e; sm += e; }
  red[t] = sm;
  __syncthreads();
  for (int st = 128; st > 0; st >>= 1) { if (t < st) red[t] += red[t + st]; __syncthreads(); }
  sm = red[0];
  if (t < DH) {
    float a = 0;
    for (int j = 0; j < SK; ++j) a += s[j] * bf2f(vb[(size_t)j * DH + t]);
    o[(size_t)row * (H * DH) + h * DH + t] = f2bf(a / sm);
  }
}

extern "C" void kernel_launch(void* const* d_in, const int* in_sizes, int n_in, void* d_out, int out_size, void* d_ws, size_t ws_size,
                              hipStream_t stream) {
  u16* out = (u16*)d_out;
  u16* q = (u16*)d_ws;
  u16* o = q + (size_t)B * SQ * H * DH;
  for (int br = 0; br < 2; ++br) {
    const u16* x = (const u16*)d_in[5 * br + 0];
    const u16* k = (const u16*)d_in[5 * br + 1];
    const u16* v = (const u16*)d_in[5 * br + 2];
    const u16* wq = (const u16*)d_in[5 * br + 3];
    const u16* wo = (const u16*)d_in[5 * br + 4];
    naive_gemm<<<dim3(H * DH / 256, B * SQ / 8), 256, 0, stream>>>(x, wq, q, B * SQ, H * DH, D, H * DH);
    naive_attn<<<dim3(B * SQ, H), 256, 0, stream>>>(q, k, v, o);
    naive_gemm<<<dim3(D / 256, B * SQ / 8), 256, 0, stream>>>(o, wo, out + br * D, B * SQ, D, H * DH, 2 * D);
  }
}
```

```cpp
#include <hip/hip_runtime.h>
#include <hip/hip_bf16.h>
typedef unsigned short u16;
constexpr int NB = 8, SQ = 160, DM = 512, NH = 32, SK = 512, DH = 128;

__global__ __launch_bounds__(256, 1) void fused_attn_block(const u16* __restrict__ xA, const u16* __restrict__ kA, const u16* __restrict__ vA,
                                                           const u16* __restrict__ wqA, const u16* __restrict__ woA, const u16* __restrict__ xB,
                                                           const u16* __restrict__ kB, const u16* __restrict__ vB, const u16* __restrict__ wqB,
                                                           const u16* __restrict__ woB, char* __restrict__ ws, u16* __restrict__ out) {
  __shared__ __attribute__((aligned(16))) char lds[163840];
  const unsigned g = blockIdx.x, xc = g & 7, u = g >> 3;
  const unsigned br = xc >> 2, hg = xc & 3, b = u >> 2, p = u & 3, h0 = 8 * hg + 2 * p;
  const char* xg = (const char*)(br ? xB : xA) + (size_t)b * (SQ * DM * 2);
  const char* wqg = (const char*)(br ? wqB : wqA) + (size_t)h0 * (DH * 2);
  const char* kg = (const char*)(br ? kB : kA) + (size_t)(b * NH + h0) * (SK * DH * 2);
  const char* vg = (const char*)(br ? vB : vA) + (size_t)(b * NH + h0) * (SK * DH * 2);
  const char* wog = (const char*)(br ? woB : woA) + (size_t)(8 * hg + 4 * (p >> 1)) * (DH * DM * 2) + (p & 1) * 512;
  char* outg = (char*)out + (size_t)b * (SQ * 2 * DM * 2) + br * (DM * 2) + (p & 1) * 512;
  const unsigned ldsb = (unsigned)(size_t)(__attribute__((address_space(3))) char*)lds;
  asm volatile(
    "v_mov_b32 v244, %8\n"
    "s_mov_b64 s[20:21], %0\n"
    "s_mov_b64 s[24:25], %1\n"
    "s_mov_b64 s[28:29], %2\n"
    "s_mov_b64 s[36:37], %3\n"
    "s_mov_b64 s[40:41], %4\n"
    "s_mov_b64 s[44:45], %5\n"
    "s_mov_b64 s[48:49], %5\n"
    "s_mov_b64 s[56:57], %5\n"
    "s_mov_b64 s[52:53], %6\n"
    "s_mov_b32 s61, %7\n"
    "v_lshrrev_b32 v245, 6, v244\n"
    "v_and_b32 v1, 63, v244\n"
    "v_readfirstlane_b32 s58, v245\n"
    "s_add_u32 s56, s56, 62914560\n"
    "s_addc_u32 s57, s57, 0\n"
    "s_nop 1\n"
    "s_lshr_b32 s59, s58, 1\n"
    "s_and_b32 s60, s58, 1\n"
    "s_cmp_lg_u32 s58, 0\n"
    "s_cbranch_scc1 HS0_SKIP\n"
    "s_lshl_b32 s64, s61, 8\n"
    "v_mov_b32 v240, s64\n"
    "global_load_dword v236, v240, s[56:57] sc1\n"
    "HS0_SKIP:\n"
    "s_sub_u32 s24, s24, 4096\n"
    "s_subb_u32 s25, s25, 0\n"
    "s_and_b32 s21, s21, 0xffff\n"
    "s_mov_b32 s22, 163840\n"
    "s_mov_b32 s23, 0x20000\n"
    "s_and_b32 s25, s25, 0xffff\n"
    "s_mov_b32 s26, 4198400\n"
    "s_mov_b32 s27, 0x20000\n"
    "s_mul_i32 s71, s58, 40960\n"
    "s_lshl_b32 s64, s60, 18\n"
    "s_lshl_b32 s65, s59, 8\n"
    "s_add_u32 s72, s64, s65\n"
    "s_mul_i32 s73, s58, 5120\n"
    "s_add_u32 s73, s73, 32768\n"
    "s_lshl_b32 s64, s59, 14\n"
    "s_lshl_b32 s65, s60, 13\n"
    "s_add_u32 s74, s64, s65\n"
    "s_mov_b32 s70, 0\n"
    "v_bfe_u32 v244, v1, 2, 3\n"
    "v_lshrrev_b32 v245, 5, v1\n"
    "v_lshlrev_b32 v245, 6, v245\n"
    "v_and_b32 v246, 3, v1\n"
    "v_lshl_add_u32 v248, v246, 4, v245\n"
    "v_lshl_add_u32 v250, v244, 13, v248\n"
    "v_add_u32 v2, 4096, v250\n"
    "v_add_u32 v3, 3200, v250\n"
    "v_add_u32 v4, 67584, v250\n"
    "v_add_u32 v5, 66688, v250\n"
    "v_lshrrev_b32 v244, 3, v1\n"
    "v_and_b32 v246, 7, v1\n"
    "v_lshrrev_b32 v247, 1, v244\n"
    "v_xor_b32 v246, v246, v247\n"
    "v_lshlrev_b32 v246, 4, v246\n"
    "v_lshl_add_u32 v249, v244, 10, v246\n"
    "s_add_u32 s64, s58, 0\n"
    "s_and_b32 s64, s64, 1\n"
    "s_lshl_b32 s64, s64, 6\n"
    "v_xor_b32 v250, s64, v249\n"
    "v_add_u32 v6, 0, v250\n"
    "s_add_u32 s64, s58, 1\n"
    "s_and_b32 s64, s64, 1\n"
    "s_lshl_b32 s64, s64, 6\n"
    "v_xor_b32 v250, s64, v249\n"
    "v_add_u32 v7, 7168, v250\n"
    "s_add_u32 s64, s58, 2\n"
    "s_and_b32 s64, s64, 1\n"
    "s_lshl_b32 s64, s64, 6\n"
    "v_xor_b32 v250, s64, v249\n"
    "v_add_u32 v8, 14336, v250\n"
    "s_add_u32 s64, s58, 3\n"
    "s_and_b32 s64, s64, 1\n"
    "s_lshl_b32 s64, s64, 6\n"
    "v_xor_b32 v250, s64, v249\n"
    "v_add_u32 v9, 21504, v250\n"
    "s_add_u32 s64, s58, 4\n"
    "s_and_b32 s64, s64, 1\n"
    "s_lshl_b32 s64, s64, 6\n"
    "v_xor_b32 v250, s64, v249\n"
    "v_add_u32 v10, 32768, v250\n"
    "s_lshl_b32 s65, s70, 19\n"
    "s_add_u32 s65, s65, s72\n"
    "s_add_u32 m0, s74, 53248\n"
    "s_lshl_b32 s64, s70, 7\n"
    "buffer_load_dwordx4 v2, s[24:27], s65 offen offset:0 lds\n"
    "buffer_load_dwordx4 v3, s[24:27], s65 offen offset:1024 lds\n"
    "buffer_load_dwordx4 v4, s[24:27], s65 offen offset:2048 lds\n"
    "buffer_load_dwordx4 v5, s[24:27], s65 offen offset:3072 lds\n"
    "s_add_u32 m0, s74, 57344\n"
    "s_add_u32 s65, s65, 131072\n"
    "buffer_load_dwordx4 v2, s[24:27], s65 offen offset:0 lds\n"
    "buffer_load_dwordx4 v3, s[24:27], s65 offen offset:1024 lds\n"
    "buffer_load_dwordx4 v4, s[24:27], s65 offen offset:2048 lds\n"
    "buffer_load_dwordx4 v5, s[24:27], s65 offen offset:3072 lds\n"
    "s_add_u32 m0, s73, 53248\n"
    "s_add_u32 s64, s64, s71\n"
    "buffer_load_dwordx4 v6, s[20:23], s64 offen offset:0 lds\n"
    "buffer_load_dwordx4 v7, s[20:23], s64 offen offset:1024 lds\n"
    "buffer_load_dwordx4 v8, s[20:23], s64 offen offset:2048 lds\n"
    "buffer_load_dwordx4 v9, s[20:23], s64 offen offset:3072 lds\n"
    "s_add_u32 m0, s73, 57344\n"
    "s_add_u32 s70, s70, 1\n"
    "buffer_load_dwordx4 v10, s[20:23], s64 offen offset:0 lds\n"
    "s_lshl_b32 s65, s70, 19\n"
    "s_add_u32 s65, s65, s72\n"
    "s_add_u32 m0, s74, 106496\n"
    "s_lshl_b32 s64, s70, 7\n"
    "buffer_load_dwordx4 v2, s[24:27], s65 offen offset:0 lds\n"
    "buffer_load_dwordx4 v3, s[24:27], s65 offen offset:1024 lds\n"
    "buffer_load_dwordx4 v4, s[24:27], s65 offen offset:2048 lds\n"
    "buffer_load_dwordx4 v5, s[24:27], s65 offen offset:3072 lds\n"
    "s_add_u32 m0, s74, 110592\n"
    "s_add_u32 s65, s65, 131072\n"
    "buffer_load_dwordx4 v2, s[24:27], s65 offen offset:0 lds\n"
    "buffer_load_dwordx4 v3, s[24:27], s65 offen offset:1024 lds\n"
    "buffer_load_dwordx4 v4, s[24:27], s65 offen offset:2048 lds\n"
    "buffer_load_dwordx4 v5, s[24:27], s65 offen offset:3072 lds\n"
    "s_add_u32 m0, s73, 106496\n"
    "s_add_u32 s64, s64, s71\n"
    "buffer_load_dwordx4 v6, s[20:23], s64 offen offset:0 lds\n"
    "buffer_load_dwordx4 v7, s[20:23], s64 offen offset:1024 lds\n"
    "buffer_load_dwordx4 v8, s[20:23], s64 offen offset:2048 lds\n"
    "buffer_load_dwordx4 v9, s[20:23], s64 offen offset:3072 lds\n"
    "s_add_u32 m0, s73, 110592\n"
    "s_add_u32 s70, s70, 1\n"
    "buffer_load_dwordx4 v10, s[20:23], s64 offen offset:0 lds\n"
    "s_and_b32 s64, s61, 3\n"
    "s_bfe_u32 s65, s61, 0x10004\n"
    "s_lshl_b32 s62, s64, 1\n"
    "s_add_u32 s62, s62, s65\n"
    "s_bfe_u32 s63, s61, 0x10003\n"
    "s_bfe_u32 s66, s61, 0x10002\n"
    "s_lshr_b32 s67, s61, 5\n"
    "s_lshl_b32 s66, s66, 3\n"
    "s_add_u32 s66, s66, s67\n"
    "s_lshl_b32 s67, s66, 5\n"
    "s_lshl_b32 s68, s62, 2\n"
    "s_add_u32 s67, s67, s68\n"
    "s_mul_i32 s67, s67, 40960\n"
    "s_add_u32 s44, s44, s67\n"
    "s_addc_u32 s45, s45, 0\n"
    "s_lshl_b32 s67, s66, 1\n"
    "s_add_u32 s67, s67, s63\n"
    "s_mul_i32 s67, s67, 655360\n"
    "s_add_u32 s67, s67, 33554432\n"
    "s_add_u32 s48, s48, s67\n"
    "s_addc_u32 s49, s49, 0\n"
    "s_lshl_b32 s63, s63, 1\n"
    "s_lshl_b32 s64, s59, 17\n"
    "s_add_u32 s28, s28, s64\n"
    "s_addc_u32 s29, s29, 0\n"
    "s_add_u32 s36, s36, s64\n"
    "s_addc_u32 s37, s37, 0\n"
    "s_sub_u32 s36, s36, 1024\n"
    "s_subb_u32 s37, s37, 0\n"
    "s_sub_u32 s40, s40, 4096\n"
    "s_subb_u32 s41, s41, 0\n"
    "s_and_b32 s29, s29, 0xffff\n"
    "s_mov_b32 s30, 131072\n"
    "s_mov_b32 s31, 0x20000\n"
    "s_and_b32 s37, s37, 0xffff\n"
    "s_mov_b32 s38, 132096\n"
    "s_mov_b32 s39, 0x20000\n"
    "s_and_b32 s41, s41, 0xffff\n"
    "s_mov_b32 s42, 528384\n"
    "s_mov_b32 s43, 0x20000\n"
    "s_and_b32 s45, s45, 0xffff\n"
    "s_mov_b32 s46, 163840\n"
    "s_mov_b32 s47, 0x20000\n"
    "s_and_b32 s49, s49, 0xffff\n"
    "s_mov_b32 s50, 655360\n"
    "s_mov_b32 s51, 0x20000\n"
    "s_and_b32 s53, s53, 0xffff\n"
    "s_mov_b32 s54, 327680\n"
    "s_mov_b32 s55, 0x20000\n"
    "s_lshl_b32 s77, s60, 12\n"
    "s_lshl_b32 s64, s59, 13\n"
    "s_add_u32 s78, s64, s77\n"
    "s_mov_b32 s83, 0x3e0293ee\n"
    "s_mov_b32 s84, 0x49800000\n"
    "s_mov_b32 s85, 0\n"
    "s_mov_b32 s81, 0\n"
    "v_lshlrev_b32 v234, 4, v1\n"
    "v_add_u32 v20, s78, v234\n"
    "v_lshrrev_b32 v244, 4, v1\n"
    "v_and_b32 v245, 15, v1\n"
    "v_xor_b32 v246, v245, v244\n"
    "v_lshlrev_b32 v246, 4, v246\n"
    "v_lshl_add_u32 v246, v244, 8, v246\n"
    "v_xor_b32 v14, 0, v246\n"
    "v_xor_b32 v15, 64, v246\n"
    "v_xor_b32 v16, 128, v246\n"
    "v_xor_b32 v17, 192, v246\n"
    "v_bfe_u32 v247, v1, 2, 3\n"
    "v_lshrrev_b32 v248, 5, v1\n"
    "v_lshlrev_b32 v248, 6, v248\n"
    "v_and_b32 v249, 3, v1\n"
    "v_lshrrev_b32 v250, 2, v247\n"
    "v_lshlrev_b32 v250, 1, v250\n"
    "v_xor_b32 v249, v249, v250\n"
    "v_lshl_add_u32 v248, v249, 4, v248\n"
    "v_lshl_add_u32 v249, v247, 8, v248\n"
    "v_add_u32 v18, 1024, v249\n"
    "v_add_u32 v19, 128, v249\n"
    "s_lshl_b32 s64, s60, 14\n"
    "s_lshl_b32 s65, s59, 8\n"
    "s_add_u32 s86, s64, s65\n"
    "s_lshl_b32 s64, s59, 13\n"
    "v_add_u32 v246, 0, v244\n"
    "v_xor_b32 v246, v246, v245\n"
    "v_lshlrev_b32 v246, 4, v246\n"
    "v_lshl_add_u32 v246, v245, 8, v246\n"
    "v_add_u32 v21, s64, v246\n"
    "v_add_u32 v246, 4, v244\n"
    "v_xor_b32 v246, v246, v245\n"
    "v_lshlrev_b32 v246, 4, v246\n"
    "v_lshl_add_u32 v246, v245, 8, v246\n"
    "v_add_u32 v22, s64, v246\n"
    "v_add_u32 v246, 8, v244\n"
    "v_xor_b32 v246, v246, v245\n"
    "v_lshlrev_b32 v246, 4, v246\n"
    "v_lshl_add_u32 v246, v245, 8, v246\n"
    "v_add_u32 v23, s64, v246\n"
    "v_add_u32 v246, 12, v244\n"
    "v_xor_b32 v246, v246, v245\n"
    "v_lshlrev_b32 v246, 4, v246\n"
    "v_lshl_add_u32 v246, v245, 8, v246\n"
    "v_add_u32 v24, s64, v246\n"
    "v_lshrrev_b32 v246, 1, v244\n"
    "v_lshlrev_b32 v246, 11, v246\n"
    "v_and_b32 v247, 1, v244\n"
    "v_lshl_add_u32 v246, v247, 8, v246\n"
    "v_lshl_add_u32 v246, v247, 5, v246\n"
    "v_bfe_u32 v248, v1, 2, 2\n"
    "v_lshl_add_u32 v246, v248, 6, v246\n"
    "v_and_b32 v248, 3, v1\n"
    "v_lshl_add_u32 v246, v248, 3, v246\n"
    "v_add_u32 v232, s64, v246\n"
    "v_xor_b32 v233, 32, v232\n"
    "v_add_u32 v25, 16384, v232\n"
    "v_add_u32 v26, 16384, v233\n"
    "v_bfe_u32 v246, v1, 2, 2\n"
    "v_lshlrev_b32 v246, 6, v246\n"
    "v_lshl_add_u32 v246, v244, 11, v246\n"
    "v_lshl_add_u32 v246, v248, 4, v246\n"
    "s_lshl_b32 s64, s59, 14\n"
    "s_add_u32 s64, s64, 53248\n"
    "v_add_u32 v11, s64, v246\n"
    "v_lshrrev_b32 v246, 1, v245\n"
    "v_xor_b32 v246, v246, v244\n"
    "v_lshlrev_b32 v246, 4, v246\n"
    "v_lshl_add_u32 v246, v245, 7, v246\n"
    "s_mul_i32 s64, s60, 10240\n"
    "s_add_u32 s64, s64, 86016\n"
    "v_add_u32 v12, s64, v246\n"
    "v_xor_b32 v13, 64, v12\n"
    "v_add_u32 v246, s62, v1\n"
    "v_and_b32 v246, 7, v246\n"
    "v_lshrrev_b32 v247, 1, v246\n"
    "v_and_b32 v246, 1, v246\n"
    "v_lshl_or_b32 v246, v246, 4, v247\n"
    "s_andn2_b32 s64, s61, 0x13\n"
    "v_or_b32 v246, s64, v246\n"
    "s_xor_b32 s64, s61, 8\n"
    "v_mov_b32 v247, s64\n"
    "v_cmp_eq_u32 vcc, 0, v1\n"
    "s_nop 1\n"
    "v_cndmask_b32 v246, v246, v247, vcc\n"
    "v_sub_u32 v247, 8, v1\n"
    "v_and_b32 v247, 7, v247\n"
    "v_lshlrev_b32 v240, 8, v246\n"
    "v_lshl_add_u32 v241, v247, 2, v240\n"
    "v_add_u32 v241, 0x10000, v241\n"
    "s_lshl_b32 s64, s61, 8\n"
    "s_add_u32 s64, s64, 0x10000\n"
    "v_lshlrev_b32 v247, 2, v1\n"
    "v_add_u32 v242, s64, v247\n"
    "v_mov_b32 v238, 0\n"
    "v_mov_b32 v239, 0\n"
    "s_waitcnt vmcnt(13)\n"
    "s_barrier\n"
    "s_cmp_lg_u32 s58, 0\n"
    "s_cbranch_scc1 HS1_SKIP\n"
    "s_mov_b64 exec, 1\n"
    "v_add_u32 v236, 1, v236\n"
    "s_lshl_b32 s69, s61, 8\n"
    "v_mov_b32 v251, s69\n"
    "s_nop 1\n"
    "v_readfirstlane_b32 s80, v236\n"
    "global_store_dword v251, v236, s[56:57] sc1\n"
    "s_mov_b64 exec, -1\n"
    "HS1_SKIP:\n"
    "ds_read_b128 v[192:195], v12 offset:0\n"
    "ds_read_b64_tr_b16 a[128:129], v11 offset:0\n"
    "ds_read_b64_tr_b16 a[130:131], v11 offset:256\n"
    "ds_read_b64_tr_b16 a[132:133], v11 offset:8\n"
    "ds_read_b64_tr_b16 a[134:135], v11 offset:264\n"
    "ds_read_b64_tr_b16 a[136:137], v11 offset:512\n"
    "ds_read_b64_tr_b16 a[138:139], v11 offset:768\n"
    "ds_read_b64_tr_b16 a[140:141], v11 offset:520\n"
    "ds_read_b64_tr_b16 a[142:143], v11 offset:776\n"
    "ds_read_b64_tr_b16 a[144:145], v11 offset:1024\n"
    "ds_read_b64_tr_b16 a[146:147], v11 offset:1280\n"
    "ds_read_b64_tr_b16 a[148:149], v11 offset:1032\n"
    "ds_read_b64_tr_b16 a[150:151], v11 offset:1288\n"
    "ds_read_b64_tr_b16 a[152:153], v11 offset:1536\n"
    "ds_read_b64_tr_b16 a[154:155], v11 offset:1792\n"
    "ds_read_b64_tr_b16 a[156:157], v11 offset:1544\n"
    "ds_read_b64_tr_b16 a[158:159], v11 offset:1800\n"
    "ds_read_b128 v[196:199], v12 offset:2048\n"
    "ds_read_b128 v[200:203], v12 offset:4096\n"
    "ds_read_b128 v[204:207], v12 offset:6144\n"
    "ds_read_b128 v[208:211], v12 offset:8192\n"
    "s_waitcnt lgkmcnt(0)\n"
    "v_mfma_f32_16x16x32_bf16 v[32:35], a[128:131], v[192:195], 0\n"
    "v_mfma_f32_16x16x32_bf16 v[36:39], a[132:135], v[192:195], 0\n"
    "ds_read_b128 v[212:215], v13 offset:0\n"
    "s_lshl_b32 s65, s70, 19\n"
    "v_mfma_f32_16x16x32_bf16 v[40:43], a[136:139], v[192:195], 0\n"
    "ds_read_b64_tr_b16 a[160:161], v11 offset:8192\n"
    "s_add_u32 s65, s65, s72\n"
    "v_mfma_f32_16x16x32_bf16 v[44:47], a[140:143], v[192:195], 0\n"
    "ds_read_b64_tr_b16 a[162:163], v11 offset:8448\n"
    "v_mfma_f32_16x16x32_bf16 v[48:51], a[144:147], v[192:195], 0\n"
    "ds_read_b64_tr_b16 a[164:165], v11 offset:8200\n"
    "s_add_u32 m0, s74, 0\n"
    "v_mfma_f32_16x16x32_bf16 v[52:55], a[148:151], v[192:195], 0\n"
    "ds_read_b64_tr_b16 a[166:167], v11 offset:8456\n"
    "s_lshl_b32 s64, s70, 7\n"
    "v_mfma_f32_16x16x32_bf16 v[56:59], a[152:155], v[192:195], 0\n"
    "v_mfma_f32_16x16x32_bf16 v[60:63], a[156:159], v[192:195], 0\n"
    "ds_read_b64_tr_b16 a[168:169], v11 offset:8704\n"
    "buffer_load_dwordx4 v2, s[24:27], s65 offen offset:0 lds\n"
    "v_mfma_f32_16x16x32_bf16 v[64:67], a[128:131], v[196:199], 0\n"
    "ds_read_b64_tr_b16 a[170:171], v11 offset:8960\n"
    "v_mfma_f32_16x16x32_bf16 v[68:71], a[132:135], v[196:199], 0\n"
    "ds_read_b64_tr_b16 a[172:173], v11 offset:8712\n"
    "buffer_load_dwordx4 v3, s[24:27], s65 offen offset:1024 lds\n"
    "v_mfma_f32_16x16x32_bf16 v[72:75], a[136:139], v[196:199], 0\n"
    "ds_read_b64_tr_b16 a[174:175], v11 offset:8968\n"
    "buffer_load_dwordx4 v4, s[24:27], s65 offen offset:2048 lds\n"
    "v_mfma_f32_16x16x32_bf16 v[76:79], a[140:143], v[196:199], 0\n"
    "v_mfma_f32_16x16x32_bf16 v[80:83], a[144:147], v[196:199], 0\n"
    "ds_read_b64_tr_b16 a[176:177], v11 offset:9216\n"
    "buffer_load_dwordx4 v5, s[24:27], s65 offen offset:3072 lds\n"
    "v_mfma_f32_16x16x32_bf16 v[84:87], a[148:151], v[196:199], 0\n"
    "ds_read_b64_tr_b16 a[178:179], v11 offset:9472\n"
    "v_mfma_f32_16x16x32_bf16 v[88:91], a[152:155], v[196:199], 0\n"
    "ds_read_b64_tr_b16 a[180:181], v11 offset:9224\n"
    "s_add_u32 m0, s74, 4096\n"
    "v_mfma_f32_16x16x32_bf16 v[92:95], a[156:159], v[196:199], 0\n"
    "ds_read_b64_tr_b16 a[182:183], v11 offset:9480\n"
    "s_add_u32 s65, s65, 131072\n"
    "v_mfma_f32_16x16x32_bf16 v[96:99], a[128:131], v[200:203], 0\n"
    "v_mfma_f32_16x16x32_bf16 v[100:103], a[132:135], v[200:203], 0\n"
    "ds_read_b64_tr_b16 a[184:185], v11 offset:9728\n"
    "buffer_load_dwordx4 v2, s[24:27], s65 offen offset:0 lds\n"
    "v_mfma_f32_16x16x32_bf16 v[104:107], a[136:139], v[200:203], 0\n"
    "ds_read_b64_tr_b16 a[186:187], v11 offset:9984\n"
    "v_mfma_f32_16x16x32_bf16 v[108:111], a[140:143], v[200:203], 0\n"
    "ds_read_b64_tr_b16 a[188:189], v11 offset:9736\n"
    "buffer_load_dwordx4 v3, s[24:27], s65 offen offset:1024 lds\n"
    "v_mfma_f32_16x16x32_bf16 v[112:115], a[144:147], v[200:203], 0\n"
    "ds_read_b64_tr_b16 a[190:191], v11 offset:9992\n"
    "buffer_load_dwordx4 v4, s[24:27], s65 offen offset:2048 lds\n"
    "v_mfma_f32_16x16x32_bf16 v[116:119], a[148:151], v[200:203], 0\n"
    "v_mfma_f32_16x16x32_bf16 v[120:123], a[152:155], v[200:203], 0\n"
    "ds_read_b128 v[216:219], v13 offset:2048\n"
    "buffer_load_dwordx4 v5, s[24:27], s65 offen offset:3072 lds\n"
    "v_mfma_f32_16x16x32_bf16 v[124:127], a[156:159], v[200:203], 0\n"
    "ds_read_b128 v[220:223], v13 offset:4096\n"
    "v_mfma_f32_16x16x32_bf16 v[128:131], a[128:131], v[204:207], 0\n"
    "ds_read_b128 v[224:227], v13 offset:6144\n"
    "s_add_u32 m0, s73, 0\n"
    "v_mfma_f32_16x16x32_bf16 v[132:135], a[132:135], v[204:207], 0\n"
    "ds_read_b128 v[228:231], v13 offset:8192\n"
    "s_add_u32 s64, s64, s71\n"
    "v_mfma_f32_16x16x32_bf16 v[136:139], a[136:139], v[204:207], 0\n"
    "v_mfma_f32_16x16x32_bf16 v[140:143], a[140:143], v[204:207], 0\n"
    "buffer_load_dwordx4 v6, s[20:23], s64 offen offset:0 lds\n"
    "v_mfma_f32_16x16x32_bf16 v[144:147], a[144:147], v[204:207], 0\n"
    "v_mfma_f32_16x16x32_bf16 v[148:151], a[148:151], v[204:207], 0\n"
    "buffer_load_dwordx4 v7, s[20:23], s64 offen offset:1024 lds\n"
    "v_mfma_f32_16x16x32_bf16 v[152:155], a[152:155], v[204:207], 0\n"
    "buffer_load_dwordx4 v8, s[20:23], s64 offen offset:2048 lds\n"
    "v_mfma_f32_16x16x32_bf16 v[156:159], a[156:159], v[204:207], 0\n"
    "v_mfma_f32_16x16x32_bf16 v[160:163], a[128:131], v[208:211], 0\n"
    "buffer_load_dwordx4 v9, s[20:23], s64 offen offset:3072 lds\n"
    "v_mfma_f32_16x16x32_bf16 v[164:167], a[132:135], v[208:211], 0\n"
    "v_mfma_f32_16x16x32_bf16 v[168:171], a[136:139], v[208:211], 0\n"
    "s_add_u32 m0, s73, 4096\n"
    "v_mfma_f32_16x16x32_bf16 v[172:175], a[140:143], v[208:211], 0\n"
    "s_add_u32 s70, s70, 1\n"
    "v_mfma_f32_16x16x32_bf16 v[176:179], a[144:147], v[208:211], 0\n"
    "v_mfma_f32_16x16x32_bf16 v[180:183], a[148:151], v[208:211], 0\n"
    "buffer_load_dwordx4 v10, s[20:23], s64 offen offset:0 lds\n"
    "v_mfma_f32_16x16x32_bf16 v[184:187], a[152:155], v[208:211], 0\n"
    "v_mfma_f32_16x16x32_bf16 v[188:191], a[156:159], v[208:211], 0\n"
    "s_waitcnt lgkmcnt(0)\n"
    "s_waitcnt vmcnt(13)\n"
    "s_barrier\n"
    "v_mfma_f32_16x16x32_bf16 v[32:35], a[160:163], v[212:215], v[32:35]\n"
    "v_add_u32 v11, 0xd000, v11\n"
    "v_add_u32 v12, 0xd000, v12\n"
    "v_add_u32 v13, 0xd000, v13\n"
    "v_mfma_f32_16x16x32_bf16 v[36:39], a[164:167], v[212:215], v[36:39]\n"
    "ds_read_b128 v[192:195], v12 offset:0\n"
    "v_mfma_f32_16x16x32_bf16 v[40:43], a[168:171], v[212:215], v[40:43]\n"
    "ds_read_b64_tr_b16 a[128:129], v11 offset:0\n"
    "s_lshl_b32 s65, s70, 19\n"
    "v_mfma_f32_16x16x32_bf16 v[44:47], a[172:175], v[212:215], v[44:47]\n"
    "ds_read_b64_tr_b16 a[130:131], v11 offset:256\n"
    "v_mfma_f32_16x16x32_bf16 v[48:51], a[176:179], v[212:215], v[48:51]\n"
    "ds_read_b64_tr_b16 a[132:133], v11 offset:8\n"
    "v_mfma_f32_16x16x32_bf16 v[52:55], a[180:183], v[212:215], v[52:55]\n"
    "ds_read_b64_tr_b16 a[134:135], v11 offset:264\n"
    "s_add_u32 s65, s65, s72\n"
    "v_mfma_f32_16x16x32_bf16 v[56:59], a[184:187], v[212:215], v[56:59]\n"
    "v_mfma_f32_16x16x32_bf16 v[60:63], a[188:191], v[212:215], v[60:63]\n"
    "ds_read_b64_tr_b16 a[136:137], v11 offset:512\n"
    "v_mfma_f32_16x16x32_bf16 v[64:67], a[160:163], v[216:219], v[64:67]\n"
    "ds_read_b64_tr_b16 a[138:139], v11 offset:768\n"
    "s_add_u32 m0, s74, 53248\n"
    "v_mfma_f32_16x16x32_bf16 v[68:71], a[164:167], v[216:219], v[68:71]\n"
    "ds_read_b64_tr_b16 a[140:141], v11 offset:520\n"
    "v_mfma_f32_16x16x32_bf16 v[72:75], a[168:171], v[216:219], v[72:75]\n"
    "ds_read_b64_tr_b16 a[142:143], v11 offset:776\n"
    "v_mfma_f32_16x16x32_bf16 v[76:79], a[172:175], v[216:219], v[76:79]\n"
    "v_mfma_f32_16x16x32_bf16 v[80:83], a[176:179], v[216:219], v[80:83]\n"
    "ds_read_b64_tr_b16 a[144:145], v11 offset:1024\n"
    "s_lshl_b32 s64, s70, 7\n"
    "v_mfma_f32_16x16x32_bf16 v[84:87], a[180:183], v[216:219], v[84:87]\n"
    "ds_read_b64_tr_b16 a[146:147], v11 offset:1280\n"
    "v_mfma_f32_16x16x32_bf16 v[88:91], a[184:187], v[216:219], v[88:91]\n"
    "ds_read_b64_tr_b16 a[148:149], v11 offset:1032\n"
    "v_mfma_f32_16x16x32_bf16 v[92:95], a[188:191], v[216:219], v[92:95]\n"
    "ds_read_b64_tr_b16 a[150:151], v11 offset:1288\n"
    "buffer_load_dwordx4 v2, s[24:27], s65 offen offset:0 lds\n"
    "v_mfma_f32_16x16x32_bf16 v[96:99], a[160:163], v[220:223], v[96:99]\n"
    "v_mfma_f32_16x16x32_bf16 v[100:103], a[164:167], v[220:223], v[100:103]\n"
    "ds_read_b64_tr_b16 a[152:153], v11 offset:1536\n"
    "v_mfma_f32_16x16x32_bf16 v[104:107], a[168:171], v[220:223], v[104:107]\n"
    "ds_read_b64_tr_b16 a[154:155], v11 offset:1792\n"
    "v_mfma_f32_16x16x32_bf16 v[108:111], a[172:175], v[220:223], v[108:111]\n"
    "ds_read_b64_tr_b16 a[156:157], v11 offset:1544\n"
    "buffer_load_dwordx4 v3, s[24:27], s65 offen offset:1024 lds\n"
    "v_mfma_f32_16x16x32_bf16 v[112:115], a[176:179], v[220:223], v[112:115]\n"
    "ds_read_b64_tr_b16 a[158:159], v11 offset:1800\n"
    "v_mfma_f32_16x16x32_bf16 v[116:119], a[180:183], v[220:223], v[116:119]\n"
    "v_mfma_f32_16x16x32_bf16 v[120:123], a[184:187], v[220:223], v[120:123]\n"
    "ds_read_b128 v[196:199], v12 offset:2048\n"
    "buffer_load_dwordx4 v4, s[24:27], s65 offen offset:2048 lds\n"
    "v_mfma_f32_16x16x32_bf16 v[124:127], a[188:191], v[220:223], v[124:127]\n"
    "ds_read_b128 v[200:203], v12 offset:4096\n"
    "v_mfma_f32_16x16x32_bf16 v[128:131], a[160:163], v[224:227], v[128:131]\n"
    "ds_read_b128 v[204:207], v12 offset:6144\n"
    "v_mfma_f32_16x16x32_bf16 v[132:135], a[164:167], v[224:227], v[132:135]\n"
    "ds_read_b128 v[208:211], v12 offset:8192\n"
    "v_mfma_f32_16x16x32_bf16 v[136:139], a[168:171], v[224:227], v[136:139]\n"
    "buffer_load_dwordx4 v5, s[24:27], s65 offen offset:3072 lds\n"
    "v_mfma_f32_16x16x32_bf16 v[140:143], a[172:175], v[224:227], v[140:143]\n"
    "v_mfma_f32_16x16x32_bf16 v[144:147], a[176:179], v[224:227], v[144:147]\n"
    "v_mfma_f32_16x16x32_bf16 v[148:151], a[180:183], v[224:227], v[148:151]\n"
    "s_add_u32 m0, s74, 57344\n"
    "v_mfma_f32_16x16x32_bf16 v[152:155], a[184:187], v[224:227], v[152:155]\n"
    "v_mfma_f32_16x16x32_bf16 v[156:159], a[188:191], v[224:227], v[156:159]\n"
    "v_mfma_f32_16x16x32_bf16 v[160:163], a[160:163], v[228:231], v[160:163]\n"
    "v_mfma_f32_16x16x32_bf16 v[164:167], a[164:167], v[228:231], v[164:167]\n"
    "s_add_u32 s65, s65, 131072\n"
    "v_mfma_f32_16x16x32_bf16 v[168:171], a[168:171], v[228:231], v[168:171]\n"
    "v_mfma_f32_16x16x32_bf16 v[172:175], a[172:175], v[228:231], v[172:175]\n"
    "v_mfma_f32_16x16x32_bf16 v[176:179], a[176:179], v[228:231], v[176:179]\n"
    "buffer_load_dwordx4 v2, s[24:27], s65 offen offset:0 lds\n"
    "v_mfma_f32_16x16x32_bf16 v[180:183], a[180:183], v[228:231], v[180:183]\n"
    "v_mfma_f32_16x16x32_bf16 v[184:187], a[184:187], v[228:231], v[184:187]\n"
    "v_mfma_f32_16x16x32_bf16 v[188:191], a[188:191], v[228:231], v[188:191]\n"
    "s_waitcnt lgkmcnt(0)\n"
    "v_mfma_f32_16x16x32_bf16 v[32:35], a[128:131], v[192:195], v[32:35]\n"
    "v_mfma_f32_16x16x32_bf16 v[36:39], a[132:135], v[192:195], v[36:39]\n"
    "ds_read_b128 v[212:215], v13 offset:0\n"
    "buffer_load_dwordx4 v3, s[24:27], s65 offen offset:1024 lds\n"
    "v_mfma_f32_16x16x32_bf16 v[40:43], a[136:139], v[192:195], v[40:43]\n"
    "ds_read_b64_tr_b16 a[160:161], v11 offset:8192\n"
    "v_mfma_f32_16x16x32_bf16 v[44:47], a[140:143], v[192:195], v[44:47]\n"
    "ds_read_b64_tr_b16 a[162:163], v11 offset:8448\n"
    "buffer_load_dwordx4 v4, s[24:27], s65 offen offset:2048 lds\n"
    "v_mfma_f32_16x16x32_bf16 v[48:51], a[144:147], v[192:195], v[48:51]\n"
    "ds_read_b64_tr_b16 a[164:165], v11 offset:8200\n"
    "v_mfma_f32_16x16x32_bf16 v[52:55], a[148:151], v[192:195], v[52:55]\n"
    "ds_read_b64_tr_b16 a[166:167], v11 offset:8456\n"
    "v_mfma_f32_16x16x32_bf16 v[56:59], a[152:155], v[192:195], v[56:59]\n"
    "buffer_load_dwordx4 v5, s[24:27], s65 offen offset:3072 lds\n"
    "v_mfma_f32_16x16x32_bf16 v[60:63], a[156:159], v[192:195], v[60:63]\n"
    "ds_read_b64_tr_b16 a[168:169], v11 offset:8704\n"
    "v_mfma_f32_16x16x32_bf16 v[64:67], a[128:131], v[196:199], v[64:67]\n"
    "ds_read_b64_tr_b16 a[170:171], v11 offset:8960\n"
    "v_mfma_f32_16x16x32_bf16 v[68:71], a[132:135], v[196:199], v[68:71]\n"
    "ds_read_b64_tr_b16 a[172:173], v11 offset:8712\n"
    "s_add_u32 m0, s73, 53248\n"
    "v_mfma_f32_16x16x32_bf16 v[72:75], a[136:139], v[196:199], v[72:75]\n"
    "ds_read_b64_tr_b16 a[174:175], v11 offset:8968\n"
    "v_mfma_f32_16x16x32_bf16 v[76:79], a[140:143], v[196:199], v[76:79]\n"
    "v_mfma_f32_16x16x32_bf16 v[80:83], a[144:147], v[196:199], v[80:83]\n"
    "ds_read_b64_tr_b16 a[176:177], v11 offset:9216\n"
    "s_add_u32 s64, s64, s71\n"
    "v_mfma_f32_16x16x32_bf16 v[84:87], a[148:151], v[196:199], v[84:87]\n"
    "ds_read_b64_tr_b16 a[178:179], v11 offset:9472\n"
    "v_mfma_f32_16x16x32_bf16 v[88:91], a[152:155], v[196:199], v[88:91]\n"
    "ds_read_b64_tr_b16 a[180:181], v11 offset:9224\n"
    "v_mfma_f32_16x16x32_bf16 v[92:95], a[156:159], v[196:199], v[92:95]\n"
    "ds_read_b64_tr_b16 a[182:183], v11 offset:9480\n"
    "buffer_load_dwordx4 v6, s[20:23], s64 offen offset:0 lds\n"
    "v_mfma_f32_16x16x32_bf16 v[96:99], a[128:131], v[200:203], v[96:99]\n"
    "v_mfma_f32_16x16x32_bf16 v[100:103], a[132:135], v[200:203], v[100:103]\n"
    "ds_read_b64_tr_b16 a[184:185], v11 offset:9728\n"
    "v_mfma_f32_16x16x32_bf16 v[104:107], a[136:139], v[200:203], v[104:107]\n"
    "ds_read_b64_tr_b16 a[186:187], v11 offset:9984\n"
    "buffer_load_dwordx4 v7, s[20:23], s64 offen offset:1024 lds\n"
    "v_mfma_f32_16x16x32_bf16 v[108:111], a[140:143], v[200:203], v[108:111]\n"
    "ds_read_b64_tr_b16 a[188:189], v11 offset:9736\n"
    "v_mfma_f32_16x16x32_bf16 v[112:115], a[144:147], v[200:203], v[112:115]\n"
    "ds_read_b64_tr_b16 a[190:191], v11 offset:9992\n"
    "buffer_load_dwordx4 v8, s[20:23], s64 offen offset:2048 lds\n"
    "v_mfma_f32_16x16x32_bf16 v[116:119], a[148:151], v[200:203], v[116:119]\n"
    "v_mfma_f32_16x16x32_bf16 v[120:123], a[152:155], v[200:203], v[120:123]\n"
    "ds_read_b128 v[216:219], v13 offset:2048\n"
    "v_mfma_f32_16x16x32_bf16 v[124:127], a[156:159], v[200:203], v[124:127]\n"
    "ds_read_b128 v[220:223], v13 offset:4096\n"
    "buffer_load_dwordx4 v9, s[20:23], s64 offen offset:3072 lds\n"
    "v_mfma_f32_16x16x32_bf16 v[128:131], a[128:131], v[204:207], v[128:131]\n"
    "ds_read_b128 v[224:227], v13 offset:6144\n"
    "v_mfma_f32_16x16x32_bf16 v[132:135], a[132:135], v[204:207], v[132:135]\n"
    "ds_read_b128 v[228:231], v13 offset:8192\n"
    "v_mfma_f32_16x16x32_bf16 v[136:139], a[136:139], v[204:207], v[136:139]\n"
    "s_add_u32 m0, s73, 57344\n"
    "v_mfma_f32_16x16x32_bf16 v[140:143], a[140:143], v[204:207], v[140:143]\n"
    "v_mfma_f32_16x16x32_bf16 v[144:147], a[144:147], v[204:207], v[144:147]\n"
    "v_mfma_f32_16x16x32_bf16 v[148:151], a[148:151], v[204:207], v[148:151]\n"
    "s_add_u32 s70, s70, 1\n"
    "v_mfma_f32_16x16x32_bf16 v[152:155], a[152:155], v[204:207], v[152:155]\n"
    "v_mfma_f32_16x16x32_bf16 v[156:159], a[156:159], v[204:207], v[156:159]\n"
    "v_mfma_f32_16x16x32_bf16 v[160:163], a[128:131], v[208:211], v[160:163]\n"
    "buffer_load_dwordx4 v10, s[20:23], s64 offen offset:0 lds\n"
    "v_mfma_f32_16x16x32_bf16 v[164:167], a[132:135], v[208:211], v[164:167]\n"
    "v_mfma_f32_16x16x32_bf16 v[168:171], a[136:139], v[208:211], v[168:171]\n"
    "v_mfma_f32_16x16x32_bf16 v[172:175], a[140:143], v[208:211], v[172:175]\n"
    "v_mfma_f32_16x16x32_bf16 v[176:179], a[144:147], v[208:211], v[176:179]\n"
    "v_mfma_f32_16x16x32_bf16 v[180:183], a[148:151], v[208:211], v[180:183]\n"
    "v_mfma_f32_16x16x32_bf16 v[184:187], a[152:155], v[208:211], v[184:187]\n"
    "v_mfma_f32_16x16x32_bf16 v[188:191], a[156:159], v[208:211], v[188:191]\n"
    "s_waitcnt lgkmcnt(0)\n"
    "s_waitcnt vmcnt(13)\n"
    "s_barrier\n"
    "s_or_b32 s69, s58, s81\n"
    "s_cmp_lg_u32 s69, 0\n"
    "s_cbranch_scc1 HSR1_SKIP\n"
    "s_mov_b64 exec, 0xff\n"
    "s_waitcnt vmcnt(13)\n"
    "global_load_dword v236, v240, s[56:57] sc1\n"
    "global_load_dword v237, v241, s[56:57] sc1\n"
    "s_mov_b64 exec, -1\n"
    "HSR1_SKIP:\n"
    "v_mfma_f32_16x16x32_bf16 v[32:35], a[160:163], v[212:215], v[32:35]\n"
    "v_add_u32 v11, 0xfffe6000, v11\n"
    "v_add_u32 v12, 0xfffe6000, v12\n"
    "v_add_u32 v13, 0xfffe6000, v13\n"
    "v_mfma_f32_16x16x32_bf16 v[36:39], a[164:167], v[212:215], v[36:39]\n"
    "ds_read_b128 v[192:195], v12 offset:0\n"
    "v_mfma_f32_16x16x32_bf16 v[40:43], a[168:171], v[212:215], v[40:43]\n"
    "ds_read_b64_tr_b16 a[128:129], v11 offset:0\n"
    "s_lshl_b32 s65, s70, 19\n"
    "v_mfma_f32_16x16x32_bf16 v[44:47], a[172:175], v[212:215], v[44:47]\n"
    "ds_read_b64_tr_b16 a[130:131], v11 offset:256\n"
    "v_mfma_f32_16x16x32_bf16 v[48:51], a[176:179], v[212:215], v[48:51]\n"
    "ds_read_b64_tr_b16 a[132:133], v11 offset:8\n"
    "v_mfma_f32_16x16x32_bf16 v[52:55], a[180:183], v[212:215], v[52:55]\n"
    "ds_read_b64_tr_b16 a[134:135], v11 offset:264\n"
    "s_add_u32 s65, s65, s72\n"
    "v_mfma_f32_16x16x32_bf16 v[56:59], a[184:187], v[212:215], v[56:59]\n"
    "v_mfma_f32_16x16x32_bf16 v[60:63], a[188:191], v[212:215], v[60:63]\n"
    "ds_read_b64_tr_b16 a[136:137], v11 offset:512\n"
    "v_mfma_f32_16x16x32_bf16 v[64:67], a[160:163], v[216:219], v[64:67]\n"
    "ds_read_b64_tr_b16 a[138:139], v11 offset:768\n"
    "s_add_u32 m0, s74, 106496\n"
    "v_mfma_f32_16x16x32_bf16 v[68:71], a[164:167], v[216:219], v[68:71]\n"
    "ds_read_b64_tr_b16 a[140:141], v11 offset:520\n"
    "v_mfma_f32_16x16x32_bf16 v[72:75], a[168:171], v[216:219], v[72:75]\n"
    "ds_read_b64_tr_b16 a[142:143], v11 offset:776\n"
    "v_mfma_f32_16x16x32_bf16 v[76:79], a[172:175], v[216:219], v[76:79]\n"
    "v_mfma_f32_16x16x32_bf16 v[80:83], a[176:179], v[216:219], v[80:83]\n"
    "ds_read_b64_tr_b16 a[144:145], v11 offset:1024\n"
    "s_lshl_b32 s64, s70, 7\n"
    "v_mfma_f32_16x16x32_bf16 v[84:87], a[180:183], v[216:219], v[84:87]\n"
    "ds_read_b64_tr_b16 a[146:147], v11 offset:1280\n"
    "v_mfma_f32_16x16x32_bf16 v[88:91], a[184:187], v[216:219], v[88:91]\n"
    "ds_read_b64_tr_b16 a[148:149], v11 offset:1032\n"
    "v_mfma_f32_16x16x32_bf16 v[92:95], a[188:191], v[216:219], v[92:95]\n"
    "ds_read_b64_tr_b16 a[150:151], v11 offset:1288\n"
    "buffer_load_dwordx4 v2, s[24:27], s65 offen offset:0 lds\n"
    "v_mfma_f32_16x16x32_bf16 v[96:99], a[160:163], v[220:223], v[96:99]\n"
    "v_mfma_f32_16x16x32_bf16 v[100:103], a[164:167], v[220:223], v[100:103]\n"
    "ds_read_b64_tr_b16 a[152:153], v11 offset:1536\n"
    "v_mfma_f32_16x16x32_bf16 v[104:107], a[168:171], v[220:223], v[104:107]\n"
    "ds_read_b64_tr_b16 a[154:155], v11 offset:1792\n"
    "v_mfma_f32_16x16x32_bf16 v[108:111], a[172:175], v[220:223], v[108:111]\n"
    "ds_read_b64_tr_b16 a[156:157], v11 offset:1544\n"
    "buffer_load_dwordx4 v3, s[24:27], s65 offen offset:1024 lds\n"
    "v_mfma_f32_16x16x32_bf16 v[112:115], a[176:179], v[220:223], v[112:115]\n"
    "ds_read_b64_tr_b16 a[158:159], v11 offset:1800\n"
    "v_mfma_f32_16x16x32_bf16 v[116:119], a[180:183], v[220:223], v[116:119]\n"
    "v_mfma_f32_16x16x32_bf16 v[120:123], a[184:187], v[220:223], v[120:123]\n"
    "ds_read_b128 v[196:199], v12 offset:2048\n"
    "buffer_load_dwordx4 v4, s[24:27], s65 offen offset:2048 lds\n"
    "v_mfma_f32_16x16x32_bf16 v[124:127], a[188:191], v[220:223], v[124:127]\n"
    "ds_read_b128 v[200:203], v12 offset:4096\n"
    "v_mfma_f32_16x16x32_bf16 v[128:131], a[160:163], v[224:227], v[128:131]\n"
    "ds_read_b128 v[204:207], v12 offset:6144\n"
    "v_mfma_f32_16x16x32_bf16 v[132:135], a[164:167], v[224:227], v[132:135]\n"
    "ds_read_b128 v[208:211], v12 offset:8192\n"
    "v_mfma_f32_16x16x32_bf16 v[136:139], a[168:171], v[224:227], v[136:139]\n"
    "buffer_load_dwordx4 v5, s[24:27], s65 offen offset:3072 lds\n"
    "v_mfma_f32_16x16x32_bf16 v[140:143], a[172:175], v[224:227], v[140:143]\n"
    "v_mfma_f32_16x16x32_bf16 v[144:147], a[176:179], v[224:227], v[144:147]\n"
    "v_mfma_f32_16x16x32_bf16 v[148:151], a[180:183], v[224:227], v[148:151]\n"
    "s_add_u32 m0, s74, 110592\n"
    "v_mfma_f32_16x16x32_bf16 v[152:155], a[184:187], v[224:227], v[152:155]\n"
    "v_mfma_f32_16x16x32_bf16 v[156:159], a[188:191], v[224:227], v[156:159]\n"
    "v_mfma_f32_16x16x32_bf16 v[160:163], a[160:163], v[228:231], v[160:163]\n"
    "v_mfma_f32_16x16x32_bf16 v[164:167], a[164:167], v[228:231], v[164:167]\n"
    "s_add_u32 s65, s65, 131072\n"
    "v_mfma_f32_16x16x32_bf16 v[168:171], a[168:171], v[228:231], v[168:171]\n"
    "v_mfma_f32_16x16x32_bf16 v[172:175], a[172:175], v[228:231], v[172:175]\n"
    "v_mfma_f32_16x16x32_bf16 v[176:179], a[176:179], v[228:231], v[176:179]\n"
    "buffer_load_dwordx4 v2, s[24:27], s65 offen offset:0 lds\n"
    "v_mfma_f32_16x16x32_bf16 v[180:183], a[180:183], v[228:231], v[180:183]\n"
    "v_mfma_f32_16x16x32_bf16 v[184:187], a[184:187], v[228:231], v[184:187]\n"
    "v_mfma_f32_16x16x32_bf16 v[188:191], a[188:191], v[228:231], v[188:191]\n"
    "s_waitcnt lgkmcnt(0)\n"
    "v_mfma_f32_16x16x32_bf16 v[32:35], a[128:131], v[192:195], v[32:35]\n"
    "v_mfma_f32_16x16x32_bf16 v[36:39], a[132:135], v[192:195], v[36:39]\n"
    "ds_read_b128 v[212:215], v13 offset:0\n"
    "buffer_load_dwordx4 v3, s[24:27], s65 offen offset:1024 lds\n"
    "v_mfma_f32_16x16x32_bf16 v[40:43], a[136:139], v[192:195], v[40:43]\n"
    "ds_read_b64_tr_b16 a[160:161], v11 offset:8192\n"
    "v_mfma_f32_16x16x32_bf16 v[44:47], a[140:143], v[192:195], v[44:47]\n"
    "ds_read_b64_tr_b16 a[162:163], v11 offset:8448\n"
    "buffer_load_dwordx4 v4, s[24:27], s65 offen offset:2048 lds\n"
    "v_mfma_f32_16x16x32_bf16 v[48:51], a[144:147], v[192:195], v[48:51]\n"
    "ds_read_b64_tr_b16 a[164:165], v11 offset:8200\n"
    "v_mfma_f32_16x16x32_bf16 v[52:55], a[148:151], v[192:195], v[52:55]\n"
    "ds_read_b64_tr_b16 a[166:167], v11 offset:8456\n"
    "v_mfma_f32_16x16x32_bf16 v[56:59], a[152:155], v[192:195], v[56:59]\n"
    "buffer_load_dwordx4 v5, s[24:27], s65 offen offset:3072 lds\n"
    "v_mfma_f32_16x16x32_bf16 v[60:63], a[156:159], v[192:195], v[60:63]\n"
    "ds_read_b64_tr_b16 a[168:169], v11 offset:8704\n"
    "v_mfma_f32_16x16x32_bf16 v[64:67], a[128:131], v[196:199], v[64:67]\n"
    "ds_read_b64_tr_b16 a[170:171], v11 offset:8960\n"
    "v_mfma_f32_16x16x32_bf16 v[68:71], a[132:135], v[196:199], v[68:71]\n"
    "ds_read_b64_tr_b16 a[172:173], v11 offset:8712\n"
    "s_add_u32 m0, s73, 106496\n"
    "v_mfma_f32_16x16x32_bf16 v[72:75], a[136:139], v[196:199], v[72:75]\n"
    "ds_read_b64_tr_b16 a[174:175], v11 offset:8968\n"
    "v_mfma_f32_16x16x32_bf16 v[76:79], a[140:143], v[196:199], v[76:79]\n"
    "v_mfma_f32_16x16x32_bf16 v[80:83], a[144:147], v[196:199], v[80:83]\n"
    "ds_read_b64_tr_b16 a[176:177], v11 offset:9216\n"
    "s_add_u32 s64, s64, s71\n"
    "v_mfma_f32_16x16x32_bf16 v[84:87], a[148:151], v[196:199], v[84:87]\n"
    "ds_read_b64_tr_b16 a[178:179], v11 offset:9472\n"
    "v_mfma_f32_16x16x32_bf16 v[88:91], a[152:155], v[196:199], v[88:91]\n"
    "ds_read_b64_tr_b16 a[180:181], v11 offset:9224\n"
    "v_mfma_f32_16x16x32_bf16 v[92:95], a[156:159], v[196:199], v[92:95]\n"
    "ds_read_b64_tr_b16 a[182:183], v11 offset:9480\n"
    "buffer_load_dwordx4 v6, s[20:23], s64 offen offset:0 lds\n"
    "v_mfma_f32_16x16x32_bf16 v[96:99], a[128:131], v[200:203], v[96:99]\n"
    "v_mfma_f32_16x16x32_bf16 v[100:103], a[132:135], v[200:203], v[100:103]\n"
    "ds_read_b64_tr_b16 a[184:185], v11 offset:9728\n"
    "v_mfma_f32_16x16x32_bf16 v[104:107], a[136:139], v[200:203], v[104:107]\n"
    "ds_read_b64_tr_b16 a[186:187], v11 offset:9984\n"
    "buffer_load_dwordx4 v7, s[20:23], s64 offen offset:1024 lds\n"
    "v_mfma_f32_16x16x32_bf16 v[108:111], a[140:143], v[200:203], v[108:111]\n"
    "ds_read_b64_tr_b16 a[188:189], v11 offset:9736\n"
    "v_mfma_f32_16x16x32_bf16 v[112:115], a[144:147], v[200:203], v[112:115]\n"
    "ds_read_b64_tr_b16 a[190:191], v11 offset:9992\n"
    "buffer_load_dwordx4 v8, s[20:23], s64 offen offset:2048 lds\n"
    "v_mfma_f32_16x16x32_bf16 v[116:119], a[148:151], v[200:203], v[116:119]\n"
    "v_mfma_f32_16x16x32_bf16 v[120:123], a[152:155], v[200:203], v[120:123]\n"
    "ds_read_b128 v[216:219], v13 offset:2048\n"
    "v_mfma_f32_16x16x32_bf16 v[124:127], a[156:159], v[200:203], v[124:127]\n"
    "ds_read_b128 v[220:223], v13 offset:4096\n"
    "buffer_load_dwordx4 v9, s[20:23], s64 offen offset:3072 lds\n"
    "v_mfma_f32_16x16x32_bf16 v[128:131], a[128:131], v[204:207], v[128:131]\n"
    "ds_read_b128 v[224:227], v13 offset:6144\n"
    "v_mfma_f32_16x16x32_bf16 v[132:135], a[132:135], v[204:207], v[132:135]\n"
    "ds_read_b128 v[228:231], v13 offset:8192\n"
    "v_mfma_f32_16x16x32_bf16 v[136:139], a[136:139], v[204:207], v[136:139]\n"
    "s_add_u32 m0, s73, 110592\n"
    "v_mfma_f32_16x16x32_bf16 v[140:143], a[140:143], v[204:207], v[140:143]\n"
    "v_mfma_f32_16x16x32_bf16 v[144:147], a[144:147], v[204:207], v[144:147]\n"
    "v_mfma_f32_16x16x32_bf16 v[148:151], a[148:151], v[204:207], v[148:151]\n"
    "s_add_u32 s70, s70, 1\n"
    "v_mfma_f32_16x16x32_bf16 v[152:155], a[152:155], v[204:207], v[152:155]\n"
    "v_mfma_f32_16x16x32_bf16 v[156:159], a[156:159], v[204:207], v[156:159]\n"
    "v_mfma_f32_16x16x32_bf16 v[160:163], a[128:131], v[208:211], v[160:163]\n"
    "buffer_load_dwordx4 v10, s[20:23], s64 offen offset:0 lds\n"
    "v_mfma_f32_16x16x32_bf16 v[164:167], a[132:135], v[208:211], v[164:167]\n"
    "v_mfma_f32_16x16x32_bf16 v[168:171], a[136:139], v[208:211], v[168:171]\n"
    "v_mfma_f32_16x16x32_bf16 v[172:175], a[140:143], v[208:211], v[172:175]\n"
    "v_mfma_f32_16x16x32_bf16 v[176:179], a[144:147], v[208:211], v[176:179]\n"
    "v_mfma_f32_16x16x32_bf16 v[180:183], a[148:151], v[208:211], v[180:183]\n"
    "v_mfma_f32_16x16x32_bf16 v[184:187], a[152:155], v[208:211], v[184:187]\n"
    "v_mfma_f32_16x16x32_bf16 v[188:191], a[156:159], v[208:211], v[188:191]\n"
    "s_waitcnt lgkmcnt(0)\n"
    "s_waitcnt vmcnt(13)\n"
    "s_barrier\n"
    "v_mfma_f32_16x16x32_bf16 v[32:35], a[160:163], v[212:215], v[32:35]\n"
    "v_add_u32 v11, 0xd000, v11\n"
    "v_add_u32 v12, 0xd000, v12\n"
    "v_add_u32 v13, 0xd000, v13\n"
    "v_mfma_f32_16x16x32_bf16 v[36:39], a[164:167], v[212:215], v[36:39]\n"
    "ds_read_b128 v[192:195], v12 offset:0\n"
    "v_mfma_f32_16x16x32_bf16 v[40:43], a[168:171], v[212:215], v[40:43]\n"
    "ds_read_b64_tr_b16 a[128:129], v11 offset:0\n"
    "s_lshl_b32 s65, s70, 19\n"
    "v_mfma_f32_16x16x32_bf16 v[44:47], a[172:175], v[212:215], v[44:47]\n"
    "ds_read_b64_tr_b16 a[130:131], v11 offset:256\n"
    "v_mfma_f32_16x16x32_bf16 v[48:51], a[176:179], v[212:215], v[48:51]\n"
    "ds_read_b64_tr_b16 a[132:133], v11 offset:8\n"
    "v_mfma_f32_16x16x32_bf16 v[52:55], a[180:183], v[212:215], v[52:55]\n"
    "ds_read_b64_tr_b16 a[134:135], v11 offset:264\n"
    "s_add_u32 s65, s65, s72\n"
    "v_mfma_f32_16x16x32_bf16 v[56:59], a[184:187], v[212:215], v[56:59]\n"
    "v_mfma_f32_16x16x32_bf16 v[60:63], a[188:191], v[212:215], v[60:63]\n"
    "ds_read_b64_tr_b16 a[136:137], v11 offset:512\n"
    "v_mfma_f32_16x16x32_bf16 v[64:67], a[160:163], v[216:219], v[64:67]\n"
    "ds_read_b64_tr_b16 a[138:139], v11 offset:768\n"
    "s_add_u32 m0, s74, 0\n"
    "v_mfma_f32_16x16x32_bf16 v[68:71], a[164:167], v[216:219], v[68:71]\n"
    "ds_read_b64_tr_b16 a[140:141], v11 offset:520\n"
    "v_mfma_f32_16x16x32_bf16 v[72:75], a[168:171], v[216:219], v[72:75]\n"
    "ds_read_b64_tr_b16 a[142:143], v11 offset:776\n"
    "v_mfma_f32_16x16x32_bf16 v[76:79], a[172:175], v[216:219], v[76:79]\n"
    "v_mfma_f32_16x16x32_bf16 v[80:83], a[176:179], v[216:219], v[80:83]\n"
    "ds_read_b64_tr_b16 a[144:145], v11 offset:1024\n"
    "s_lshl_b32 s64, s70, 7\n"
    "v_mfma_f32_16x16x32_bf16 v[84:87], a[180:183], v[216:219], v[84:87]\n"
    "ds_read_b64_tr_b16 a[146:147], v11 offset:1280\n"
    "v_mfma_f32_16x16x32_bf16 v[88:91], a[184:187], v[216:219], v[88:91]\n"
    "ds_read_b64_tr_b16 a[148:149], v11 offset:1032\n"
    "v_mfma_f32_16x16x32_bf16 v[92:95], a[188:191], v[216:219], v[92:95]\n"
    "ds_read_b64_tr_b16 a[150:151], v11 offset:1288\n"
    "buffer_load_dwordx4 v2, s[24:27], s65 offen offset:0 lds\n"
    "v_mfma_f32_16x16x32_bf16 v[96:99], a[160:163], v[220:223], v[96:99]\n"
    "v_mfma_f32_16x16x32_bf16 v[100:103], a[164:167], v[220:223], v[100:103]\n"
    "ds_read_b64_tr_b16 a[152:153], v11 offset:1536\n"
    "v_mfma_f32_16x16x32_bf16 v[104:107], a[168:171], v[220:223], v[104:107]\n"
    "ds_read_b64_tr_b16 a[154:155], v11 offset:1792\n"
    "v_mfma_f32_16x16x32_bf16 v[108:111], a[172:175], v[220:223], v[108:111]\n"
    "ds_read_b64_tr_b16 a[156:157], v11 offset:1544\n"
    "buffer_load_dwordx4 v3, s[24:27], s65 offen offset:1024 lds\n"
    "v_mfma_f32_16x16x32_bf16 v[112:115], a[176:179], v[220:223], v[112:115]\n"
    "ds_read_b64_tr_b16 a[158:159], v11 offset:1800\n"
    "v_mfma_f32_16x16x32_bf16 v[116:119], a[180:183], v[220:223], v[116:119]\n"
    "v_mfma_f32_16x16x32_bf16 v[120:123], a[184:187], v[220:223], v[120:123]\n"
    "ds_read_b128 v[196:199], v12 offset:2048\n"
    "buffer_load_dwordx4 v4, s[24:27], s65 offen offset:2048 lds\n"
    "v_mfma_f32_16x16x32_bf16 v[124:127], a[188:191], v[220:223], v[124:127]\n"
    "ds_read_b128 v[200:203], v12 offset:4096\n"
    "v_mfma_f32_16x16x32_bf16 v[128:131], a[160:163], v[224:227], v[128:131]\n"
    "ds_read_b128 v[204:207], v12 offset:6144\n"
    "v_mfma_f32_16x16x32_bf16 v[132:135], a[164:167], v[224:227], v[132:135]\n"
    "ds_read_b128 v[208:211], v12 offset:8192\n"
    "v_mfma_f32_16x16x32_bf16 v[136:139], a[168:171], v[224:227], v[136:139]\n"
    "buffer_load_dwordx4 v5, s[24:27], s65 offen offset:3072 lds\n"
    "v_mfma_f32_16x16x32_bf16 v[140:143], a[172:175], v[224:227], v[140:143]\n"
    "v_mfma_f32_16x16x32_bf16 v[144:147], a[176:179], v[224:227], v[144:147]\n"
    "v_mfma_f32_16x16x32_bf16 v[148:151], a[180:183], v[224:227], v[148:151]\n"
    "s_add_u32 m0, s74, 4096\n"
    "v_mfma_f32_16x16x32_bf16 v[152:155], a[184:187], v[224:227], v[152:155]\n"
    "v_mfma_f32_16x16x32_bf16 v[156:159], a[188:191], v[224:227], v[156:159]\n"
    "v_mfma_f32_16x16x32_bf16 v[160:163], a[160:163], v[228:231], v[160:163]\n"
    "v_mfma_f32_16x16x32_bf16 v[164:167], a[164:167], v[228:231], v[164:167]\n"
    "s_add_u32 s65, s65, 131072\n"
    "v_mfma_f32_16x16x32_bf16 v[168:171], a[168:171], v[228:231], v[168:171]\n"
    "v_mfma_f32_16x16x32_bf16 v[172:175], a[172:175], v[228:231], v[172:175]\n"
    "v_mfma_f32_16x16x32_bf16 v[176:179], a[176:179], v[228:231], v[176:179]\n"
    "buffer_load_dwordx4 v2, s[24:27], s65 offen offset:0 lds\n"
    "v_mfma_f32_16x16x32_bf16 v[180:183], a[180:183], v[228:231], v[180:183]\n"
    "v_mfma_f32_16x16x32_bf16 v[184:187], a[184:187], v[228:231], v[184:187]\n"
    "v_mfma_f32_16x16x32_bf16 v[188:191], a[188:191], v[228:231], v[188:191]\n"
    "s_waitcnt lgkmcnt(0)\n"
    "v_mfma_f32_16x16x32_bf16 v[32:35], a[128:131], v[192:195], v[32:35]\n"
    "v_mfma_f32_16x16x32_bf16 v[36:39], a[132:135], v[192:195], v[36:39]\n"
    "ds_read_b128 v[212:215], v13 offset:0\n"
    "buffer_load_dwordx4 v3, s[24:27], s65 offen offset:1024 lds\n"
    "v_mfma_f32_16x16x32_bf16 v[40:43], a[136:139], v[192:195], v[40:43]\n"
    "ds_read_b64_tr_b16 a[160:161], v11 offset:8192\n"
    "v_mfma_f32_16x16x32_bf16 v[44:47], a[140:143], v[192:195], v[44:47]\n"
    "ds_read_b64_tr_b16 a[162:163], v11 offset:8448\n"
    "buffer_load_dwordx4 v4, s[24:27], s65 offen offset:2048 lds\n"
    "v_mfma_f32_16x16x32_bf16 v[48:51], a[144:147], v[192:195], v[48:51]\n"
    "ds_read_b64_tr_b16 a[164:165], v11 offset:8200\n"
    "v_mfma_f32_16x16x32_bf16 v[52:55], a[148:151], v[192:195], v[52:55]\n"
    "ds_read_b64_tr_b16 a[166:167], v11 offset:8456\n"
    "v_mfma_f32_16x16x32_bf16 v[56:59], a[152:155], v[192:195], v[56:59]\n"
    "buffer_load_dwordx4 v5, s[24:27], s65 offen offset:3072 lds\n"
    "v_mfma_f32_16x16x32_bf16 v[60:63], a[156:159], v[192:195], v[60:63]\n"
    "ds_read_b64_tr_b16 a[168:169], v11 offset:8704\n"
    "v_mfma_f32_16x16x32_bf16 v[64:67], a[128:131], v[196:199], v[64:67]\n"
    "ds_read_b64_tr_b16 a[170:171], v11 offset:8960\n"
    "v_mfma_f32_16x16x32_bf16 v[68:71], a[132:135], v[196:199], v[68:71]\n"
    "ds_read_b64_tr_b16 a[172:173], v11 offset:8712\n"
    "s_add_u32 m0, s73, 0\n"
    "v_mfma_f32_16x16x32_bf16 v[72:75], a[136:139], v[196:199], v[72:75]\n"
    "ds_read_b64_tr_b16 a[174:175], v11 offset:8968\n"
    "v_mfma_f32_16x16x32_bf16 v[76:79], a[140:143], v[196:199], v[76:79]\n"
    "v_mfma_f32_16x16x32_bf16 v[80:83], a[144:147], v[196:199], v[80:83]\n"
    "ds_read_b64_tr_b16 a[176:177], v11 offset:9216\n"
    "s_add_u32 s64, s64, s71\n"
    "v_mfma_f32_16x16x32_bf16 v[84:87], a[148:151], v[196:199], v[84:87]\n"
    "ds_read_b64_tr_b16 a[178:179], v11 offset:9472\n"
    "v_mfma_f32_16x16x32_bf16 v[88:91], a[152:155], v[196:199], v[88:91]\n"
    "ds_read_b64_tr_b16 a[180:181], v11 offset:9224\n"
    "v_mfma_f32_16x16x32_bf16 v[92:95], a[156:159], v[196:199], v[92:95]\n"
    "ds_read_b64_tr_b16 a[182:183], v11 offset:9480\n"
    "buffer_load_dwordx4 v6, s[20:23], s64 offen offset:0 lds\n"
    "v_mfma_f32_16x16x32_bf16 v[96:99], a[128:131], v[200:203], v[96:99]\n"
    "v_mfma_f32_16x16x32_bf16 v[100:103], a[132:135], v[200:203], v[100:103]\n"
    "ds_read_b64_tr_b16 a[184:185], v11 offset:9728\n"
    "v_mfma_f32_16x16x32_bf16 v[104:107], a[136:139], v[200:203], v[104:107]\n"
    "ds_read_b64_tr_b16 a[186:187], v11 offset:9984\n"
    "buffer_load_dwordx4 v7, s[20:23], s64 offen offset:1024 lds\n"
    "v_mfma_f32_16x16x32_bf16 v[108:111], a[140:143], v[200:203], v[108:111]\n"
    "ds_read_b64_tr_b16 a[188:189], v11 offset:9736\n"
    "v_mfma_f32_16x16x32_bf16 v[112:115], a[144:147], v[200:203], v[112:115]\n"
    "ds_read_b64_tr_b16 a[190:191], v11 offset:9992\n"
    "buffer_load_dwordx4 v8, s[20:23], s64 offen offset:2048 lds\n"
    "v_mfma_f32_16x16x32_bf16 v[116:119], a[148:151], v[200:203], v[116:119]\n"
    "v_mfma_f32_16x16x32_bf16 v[120:123], a[152:155], v[200:203], v[120:123]\n"
    "ds_read_b128 v[216:219], v13 offset:2048\n"
    "v_mfma_f32_16x16x32_bf16 v[124:127], a[156:159], v[200:203], v[124:127]\n"
    "ds_read_b128 v[220:223], v13 offset:4096\n"
    "buffer_load_dwordx4 v9, s[20:23], s64 offen offset:3072 lds\n"
    "v_mfma_f32_16x16x32_bf16 v[128:131], a[128:131], v[204:207], v[128:131]\n"
    "ds_read_b128 v[224:227], v13 offset:6144\n"
    "v_mfma_f32_16x16x32_bf16 v[132:135], a[132:135], v[204:207], v[132:135]\n"
    "ds_read_b128 v[228:231], v13 offset:8192\n"
    "v_mfma_f32_16x16x32_bf16 v[136:139], a[136:139], v[204:207], v[136:139]\n"
    "s_add_u32 m0, s73, 4096\n"
    "v_mfma_f32_16x16x32_bf16 v[140:143], a[140:143], v[204:207], v[140:143]\n"
    "v_mfma_f32_16x16x32_bf16 v[144:147], a[144:147], v[204:207], v[144:147]\n"
    "v_mfma_f32_16x16x32_bf16 v[148:151], a[148:151], v[204:207], v[148:151]\n"
    "s_add_u32 s70, s70, 1\n"
    "v_mfma_f32_16x16x32_bf16 v[152:155], a[152:155], v[204:207], v[152:155]\n"
    "v_mfma_f32_16x16x32_bf16 v[156:159], a[156:159], v[204:207], v[156:159]\n"
    "v_mfma_f32_16x16x32_bf16 v[160:163], a[128:131], v[208:211], v[160:163]\n"
    "buffer_load_dwordx4 v10, s[20:23], s64 offen offset:0 lds\n"
    "v_mfma_f32_16x16x32_bf16 v[164:167], a[132:135], v[208:211], v[164:167]\n"
    "v_mfma_f32_16x16x32_bf16 v[168:171], a[136:139], v[208:211], v[168:171]\n"
    "v_mfma_f32_16x16x32_bf16 v[172:175], a[140:143], v[208:211], v[172:175]\n"
    "v_mfma_f32_16x16x32_bf16 v[176:179], a[144:147], v[208:211], v[176:179]\n"
    "v_mfma_f32_16x16x32_bf16 v[180:183], a[148:151], v[208:211], v[180:183]\n"
    "v_mfma_f32_16x16x32_bf16 v[184:187], a[152:155], v[208:211], v[184:187]\n"
    "v_mfma_f32_16x16x32_bf16 v[188:191], a[156:159], v[208:211], v[188:191]\n"
    "s_waitcnt lgkmcnt(0)\n"
    "s_waitcnt vmcnt(13)\n"
    "s_barrier\n"
    "s_or_b32 s69, s58, s81\n"
    "s_cmp_lg_u32 s69, 0\n"
    "s_cbranch_scc1 HSR2_SKIP\n"
    "s_mov_b64 exec, 0xff\n"
    "s_waitcnt vmcnt(13)\n"
    "v_cmp_eq_u32 vcc, 1, v238\n"
    "s_nop 2\n"
    "v_cndmask_b32 v239, v239, v236, vcc\n"
    "v_cndmask_b32 v251, v238, 2, vcc\n"
    "v_cmp_eq_u32 s[88:89], s80, v237\n"
    "v_cmp_eq_u32 vcc, 0, v251\n"
    "s_nop 2\n"
    "s_and_b64 vcc, vcc, s[88:89]\n"
    "v_cndmask_b32 v238, v251, 1, vcc\n"
    "global_store_dword v242, v236, s[56:57] sc1\n"
    "v_cmp_eq_u32 vcc, 2, v238\n"
    "s_nop 2\n"
    "s_cmp_eq_u64 vcc, 0xff\n"
    "s_cselect_b32 s81, 1, 0\n"
    "global_load_dword v236, v240, s[56:57] sc1\n"
    "global_load_dword v237, v241, s[56:57] sc1\n"
    "s_mov_b64 exec, -1\n"
    "HSR2_SKIP:\n"
    "v_mfma_f32_16x16x32_bf16 v[32:35], a[160:163], v[212:215], v[32:35]\n"
    "v_add_u32 v11, 0xd000, v11\n"
    "v_add_u32 v12, 0xd000, v12\n"
    "v_add_u32 v13, 0xd000, v13\n"
    "v_mfma_f32_16x16x32_bf16 v[36:39], a[164:167], v[212:215], v[36:39]\n"
    "ds_read_b128 v[192:195], v12 offset:0\n"
    "v_mfma_f32_16x16x32_bf16 v[40:43], a[168:171], v[212:215], v[40:43]\n"
    "ds_read_b64_tr_b16 a[128:129], v11 offset:0\n"
    "s_lshl_b32 s65, s70, 19\n"
    "v_mfma_f32_16x16x32_bf16 v[44:47], a[172:175], v[212:215], v[44:47]\n"
    "ds_read_b64_tr_b16 a[130:131], v11 offset:256\n"
    "v_mfma_f32_16x16x32_bf16 v[48:51], a[176:179], v[212:215], v[48:51]\n"
    "ds_read_b64_tr_b16 a[132:133], v11 offset:8\n"
    "v_mfma_f32_16x16x32_bf16 v[52:55], a[180:183], v[212:215], v[52:55]\n"
    "ds_read_b64_tr_b16 a[134:135], v11 offset:264\n"
    "s_add_u32 s65, s65, s72\n"
    "v_mfma_f32_16x16x32_bf16 v[56:59], a[184:187], v[212:215], v[56:59]\n"
    "v_mfma_f32_16x16x32_bf16 v[60:63], a[188:191], v[212:215], v[60:63]\n"
    "ds_read_b64_tr_b16 a[136:137], v11 offset:512\n"
    "v_mfma_f32_16x16x32_bf16 v[64:67], a[160:163], v[216:219], v[64:67]\n"
    "ds_read_b64_tr_b16 a[138:139], v11 offset:768\n"
    "s_add_u32 m0, s74, 53248\n"
    "v_mfma_f32_16x16x32_bf16 v[68:71], a[164:167], v[216:219], v[68:71]\n"
    "ds_read_b64_tr_b16 a[140:141], v11 offset:520\n"
    "v_mfma_f32_16x16x32_bf16 v[72:75], a[168:171], v[216:219], v[72:75]\n"
    "ds_read_b64_tr_b16 a[142:143], v11 offset:776\n"
    "v_mfma_f32_16x16x32_bf16 v[76:79], a[172:175], v[216:219], v[76:79]\n"
    "v_mfma_f32_16x16x32_bf16 v[80:83], a[176:179], v[216:219], v[80:83]\n"
    "ds_read_b64_tr_b16 a[144:145], v11 offset:1024\n"
    "s_lshl_b32 s64, s70, 7\n"
    "v_mfma_f32_16x16x32_bf16 v[84:87], a[180:183], v[216:219], v[84:87]\n"
    "ds_read_b64_tr_b16 a[146:147], v11 offset:1280\n"
    "v_mfma_f32_16x16x32_bf16 v[88:91], a[184:187], v[216:219], v[88:91]\n"
    "ds_read_b64_tr_b16 a[148:149], v11 offset:1032\n"
    "v_mfma_f32_16x16x32_bf16 v[92:95], a[188:191], v[216:219], v[92:95]\n"
    "ds_read_b64_tr_b16 a[150:151], v11 offset:1288\n"
    "buffer_load_dwordx4 v2, s[24:27], s65 offen offset:0 lds\n"
    "v_mfma_f32_16x16x32_bf16 v[96:99], a[160:163], v[220:223], v[96:99]\n"
    "v_mfma_f32_16x16x32_bf16 v[100:103], a[164:167], v[220:223], v[100:103]\n"
    "ds_read_b64_tr_b16 a[152:153], v11 offset:1536\n"
    "v_mfma_f32_16x16x32_bf16 v[104:107], a[168:171], v[220:223], v[104:107]\n"
    "ds_read_b64_tr_b16 a[154:155], v11 offset:1792\n"
    "v_mfma_f32_16x16x32_bf16 v[108:111], a[172:175], v[220:223], v[108:111]\n"
    "ds_read_b64_tr_b16 a[156:157], v11 offset:1544\n"
    "buffer_load_dwordx4 v3, s[24:27], s65 offen offset:1024 lds\n"
    "v_mfma_f32_16x16x32_bf16 v[112:115], a[176:179], v[220:223], v[112:115]\n"
    "ds_read_b64_tr_b16 a[158:159], v11 offset:1800\n"
    "v_mfma_f32_16x16x32_bf16 v[116:119], a[180:183], v[220:223], v[116:119]\n"
    "v_mfma_f32_16x16x32_bf16 v[120:123], a[184:187], v[220:223], v[120:123]\n"
    "ds_read_b128 v[196:199], v12 offset:2048\n"
    "buffer_load_dwordx4 v4, s[24:27], s65 offen offset:2048 lds\n"
    "v_mfma_f32_16x16x32_bf16 v[124:127], a[188:191], v[220:223], v[124:127]\n"
    "ds_read_b128 v[200:203], v12 offset:4096\n"
    "v_mfma_f32_16x16x32_bf16 v[128:131], a[160:163], v[224:227], v[128:131]\n"
    "ds_read_b128 v[204:207], v12 offset:6144\n"
    "v_mfma_f32_16x16x32_bf16 v[132:135], a[164:167], v[224:227], v[132:135]\n"
    "ds_read_b128 v[208:211], v12 offset:8192\n"
    "v_mfma_f32_16x16x32_bf16 v[136:139], a[168:171], v[224:227], v[136:139]\n"
    "buffer_load_dwordx4 v5, s[24:27], s65 offen offset:3072 lds\n"
    "v_mfma_f32_16x16x32_bf16 v[140:143], a[172:175], v[224:227], v[140:143]\n"
    "v_mfma_f32_16x16x32_bf16 v[144:147], a[176:179], v[224:227], v[144:147]\n"
    "v_mfma_f32_16x16x32_bf16 v[148:151], a[180:183], v[224:227], v[148:151]\n"
    "s_add_u32 m0, s74, 57344\n"
    "v_mfma_f32_16x16x32_bf16 v[152:155], a[184:187], v[224:227], v[152:155]\n"
    "v_mfma_f32_16x16x32_bf16 v[156:159], a[188:191], v[224:227], v[156:159]\n"
    "v_mfma_f32_16x16x32_bf16 v[160:163], a[160:163], v[228:231], v[160:163]\n"
    "v_mfma_f32_16x16x32_bf16 v[164:167], a[164:167], v[228:231], v[164:167]\n"
    "s_add_u32 s65, s65, 131072\n"
    "v_mfma_f32_16x16x32_bf16 v[168:171], a[168:171], v[228:231], v[168:171]\n"
    "v_mfma_f32_16x16x32_bf16 v[172:175], a[172:175], v[228:231], v[172:175]\n"
    "v_mfma_f32_16x16x32_bf16 v[176:179], a[176:179], v[228:231], v[176:179]\n"
    "buffer_load_dwordx4 v2, s[24:27], s65 offen offset:0 lds\n"
    "v_mfma_f32_16x16x32_bf16 v[180:183], a[180:183], v[228:231], v[180:183]\n"
    "v_mfma_f32_16x16x32_bf16 v[184:187], a[184:187], v[228:231], v[184:187]\n"
    "v_mfma_f32_16x16x32_bf16 v[188:191], a[188:191], v[228:231], v[188:191]\n"
    "s_waitcnt lgkmcnt(0)\n"
    "v_mfma_f32_16x16x32_bf16 v[32:35], a[128:131], v[192:195], v[32:35]\n"
    "v_mfma_f32_16x16x32_bf16 v[36:39], a[132:135], v[192:195], v[36:39]\n"
    "ds_read_b128 v[212:215], v13 offset:0\n"
    "buffer_load_dwordx4 v3, s[24:27], s65 offen offset:1024 lds\n"
    "v_mfma_f32_16x16x32_bf16 v[40:43], a[136:139], v[192:195], v[40:43]\n"
    "ds_read_b64_tr_b16 a[160:161], v11 offset:8192\n"
    "v_mfma_f32_16x16x32_bf16 v[44:47], a[140:143], v[192:195], v[44:47]\n"
    "ds_read_b64_tr_b16 a[162:163], v11 offset:8448\n"
    "buffer_load_dwordx4 v4, s[24:27], s65 offen offset:2048 lds\n"
    "v_mfma_f32_16x16x32_bf16 v[48:51], a[144:147], v[192:195], v[48:51]\n"
    "ds_read_b64_tr_b16 a[164:165], v11 offset:8200\n"
    "v_mfma_f32_16x16x32_bf16 v[52:55], a[148:151], v[192:195], v[52:55]\n"
    "ds_read_b64_tr_b16 a[166:167], v11 offset:8456\n"
    "v_mfma_f32_16x16x32_bf16 v[56:59], a[152:155], v[192:195], v[56:59]\n"
    "buffer_load_dwordx4 v5, s[24:27], s65 offen offset:3072 lds\n"
    "v_mfma_f32_16x16x32_bf16 v[60:63], a[156:159], v[192:195], v[60:63]\n"
    "ds_read_b64_tr_b16 a[168:169], v11 offset:8704\n"
    "v_mfma_f32_16x16x32_bf16 v[64:67], a[128:131], v[196:199], v[64:67]\n"
    "ds_read_b64_tr_b16 a[170:171], v11 offset:8960\n"
    "v_mfma_f32_16x16x32_bf16 v[68:71], a[132:135], v[196:199], v[68:71]\n"
    "ds_read_b64_tr_b16 a[172:173], v11 offset:8712\n"
    "s_add_u32 m0, s73, 53248\n"
    "v_mfma_f32_16x16x32_bf16 v[72:75], a[136:139], v[196:199], v[72:75]\n"
    "ds_read_b64_tr_b16 a[174:175], v11 offset:8968\n"
    "v_mfma_f32_16x16x32_bf16 v[76:79], a[140:143], v[196:199], v[76:79]\n"
    "v_mfma_f32_16x16x32_bf16 v[80:83], a[144:147], v[196:199], v[80:83]\n"
    "ds_read_b64_tr_b16 a[176:177], v11 offset:9216\n"
    "s_add_u32 s64, s64, s71\n"
    "v_mfma_f32_16x16x32_bf16 v[84:87], a[148:151], v[196:199], v[84:87]\n"
    "ds_read_b64_tr_b16 a[178:179], v11 offset:9472\n"
    "v_mfma_f32_16x16x32_bf16 v[88:91], a[152:155], v[196:199], v[88:91]\n"
    "ds_read_b64_tr_b16 a[180:181], v11 offset:9224\n"
    "v_mfma_f32_16x16x32_bf16 v[92:95], a[156:159], v[196:199], v[92:95]\n"
    "ds_read_b64_tr_b16 a[182:183], v11 offset:9480\n"
    "buffer_load_dwordx4 v6, s[20:23], s64 offen offset:0 lds\n"
    "v_mfma_f32_16x16x32_bf16 v[96:99], a[128:131], v[200:203], v[96:99]\n"
    "v_mfma_f32_16x16x32_bf16 v[100:103], a[132:135], v[200:203], v[100:103]\n"
    "ds_read_b64_tr_b16 a[184:185], v11 offset:9728\n"
    "v_mfma_f32_16x16x32_bf16 v[104:107], a[136:139], v[200:203], v[104:107]\n"
    "ds_read_b64_tr_b16 a[186:187], v11 offset:9984\n"
    "buffer_load_dwordx4 v7, s[20:23], s64 offen offset:1024 lds\n"
    "v_mfma_f32_16x16x32_bf16 v[108:111], a[140:143], v[200:203], v[108:111]\n"
    "ds_read_b64_tr_b16 a[188:189], v11 offset:9736\n"
    "v_mfma_f32_16x16x32_bf16 v[112:115], a[144:147], v[200:203], v[112:115]\n"
    "ds_read_b64_tr_b16 a[190:191], v11 offset:9992\n"
    "buffer_load_dwordx4 v8, s[20:23], s64 offen offset:2048 lds\n"
    "v_mfma_f32_16x16x32_bf16 v[116:119], a[148:151], v[200:203], v[116:119]\n"
    "v_mfma_f32_16x16x32_bf16 v[120:123], a[152:155], v[200:203], v[120:123]\n"
    "ds_read_b128 v[216:219], v13 offset:2048\n"
    "v_mfma_f32_16x16x32_bf16 v[124:127], a[156:159], v[200:203], v[124:127]\n"
    "ds_read_b128 v[220:223], v13 offset:4096\n"
    "buffer_load_dwordx4 v9, s[20:23], s64 offen offset:3072 lds\n"
    "v_mfma_f32_16x16x32_bf16 v[128:131], a[128:131], v[204:207], v[128:131]\n"
    "ds_read_b128 v[224:227], v13 offset:6144\n"
    "v_mfma_f32_16x16x32_bf16 v[132:135], a[132:135], v[204:207], v[132:135]\n"
    "ds_read_b128 v[228:231], v13 offset:8192\n"
    "v_mfma_f32_16x16x32_bf16 v[136:139], a[136:139], v[204:207], v[136:139]\n"
    "s_add_u32 m0, s73, 57344\n"
    "v_mfma_f32_16x16x32_bf16 v[140:143], a[140:143], v[204:207], v[140:143]\n"
    "v_mfma_f32_16x16x32_bf16 v[144:147], a[144:147], v[204:207], v[144:147]\n"
    "v_mfma_f32_16x16x32_bf16 v[148:151], a[148:151], v[204:207], v[148:151]\n"
    "s_add_u32 s70, s70, 1\n"
    "v_mfma_f32_16x16x32_bf16 v[152:155], a[152:155], v[204:207], v[152:155]\n"
    "v_mfma_f32_16x16x32_bf16 v[156:159], a[156:159], v[204:207], v[156:159]\n"
    "v_mfma_f32_16x16x32_bf16 v[160:163], a[128:131], v[208:211], v[160:163]\n"
    "buffer_load_dwordx4 v10, s[20:23], s64 offen offset:0 lds\n"
    "v_mfma_f32_16x16x32_bf16 v[164:167], a[132:135], v[208:211], v[164:167]\n"
    "v_mfma_f32_16x16x32_bf16 v[168:171], a[136:139], v[208:211], v[168:171]\n"
    "v_mfma_f32_16x16x32_bf16 v[172:175], a[140:143], v[208:211], v[172:175]\n"
    "v_mfma_f32_16x16x32_bf16 v[176:179], a[144:147], v[208:211], v[176:179]\n"
    "v_mfma_f32_16x16x32_bf16 v[180:183], a[148:151], v[208:211], v[180:183]\n"
    "v_mfma_f32_16x16x32_bf16 v[184:187], a[152:155], v[208:211], v[184:187]\n"
    "v_mfma_f32_16x16x32_bf16 v[188:191], a[156:159], v[208:211], v[188:191]\n"
    "s_waitcnt lgkmcnt(0)\n"
    "s_waitcnt vmcnt(13)\n"
    "s_barrier\n"
    "v_mfma_f32_16x16x32_bf16 v[32:35], a[160:163], v[212:215], v[32:35]\n"
    "v_add_u32 v11, 0xfffe6000, v11\n"
    "v_add_u32 v12, 0xfffe6000, v12\n"
    "v_add_u32 v13, 0xfffe6000, v13\n"
    "v_mfma_f32_16x16x32_bf16 v[36:39], a[164:167], v[212:215], v[36:39]\n"
    "ds_read_b128 v[192:195], v12 offset:0\n"
    "v_mfma_f32_16x16x32_bf16 v[40:43], a[168:171], v[212:215], v[40:43]\n"
    "ds_read_b64_tr_b16 a[128:129], v11 offset:0\n"
    "s_lshl_b32 s65, s70, 19\n"
    "v_mfma_f32_16x16x32_bf16 v[44:47], a[172:175], v[212:215], v[44:47]\n"
    "ds_read_b64_tr_b16 a[130:131], v11 offset:256\n"
    "v_mfma_f32_16x16x32_bf16 v[48:51], a[176:179], v[212:215], v[48:51]\n"
    "ds_read_b64_tr_b16 a[132:133], v11 offset:8\n"
    "v_mfma_f32_16x16x32_bf16 v[52:55], a[180:183], v[212:215], v[52:55]\n"
    "ds_read_b64_tr_b16 a[134:135], v11 offset:264\n"
    "s_add_u32 s65, s65, s72\n"
    "v_mfma_f32_16x16x32_bf16 v[56:59], a[184:187], v[212:215], v[56:59]\n"
    "v_mfma_f32_16x16x32_bf16 v[60:63], a[188:191], v[212:215], v[60:63]\n"
    "ds_read_b64_tr_b16 a[136:137], v11 offset:512\n"
    "v_mfma_f32_16x16x32_bf16 v[64:67], a[160:163], v[216:219], v[64:67]\n"
    "ds_read_b64_tr_b16 a[138:139], v11 offset:768\n"
    "s_add_u32 m0, s74, 106496\n"
    "v_mfma_f32_16x16x32_bf16 v[68:71], a[164:167], v[216:219], v[68:71]\n"
    "ds_read_b64_tr_b16 a[140:141], v11 offset:520\n"
    "v_mfma_f32_16x16x32_bf16 v[72:75], a[168:171], v[216:219], v[72:75]\n"
    "ds_read_b64_tr_b16 a[142:143], v11 offset:776\n"
    "v_mfma_f32_16x16x32_bf16 v[76:79], a[172:175], v[216:219], v[76:79]\n"
    "v_mfma_f32_16x16x32_bf16 v[80:83], a[176:179], v[216:219], v[80:83]\n"
    "ds_read_b64_tr_b16 a[144:145], v11 offset:1024\n"
    "s_lshl_b32 s64, s70, 7\n"
    "v_mfma_f32_16x16x32_bf16 v[84:87], a[180:183], v[216:219], v[84:87]\n"
    "ds_read_b64_tr_b16 a[146:147], v11 offset:1280\n"
    "v_mfma_f32_16x16x32_bf16 v[88:91], a[184:187], v[216:219], v[88:91]\n"
    "ds_read_b64_tr_b16 a[148:149], v11 offset:1032\n"
    "v_mfma_f32_16x16x32_bf16 v[92:95], a[188:191], v[216:219], v[92:95]\n"
    "ds_read_b64_tr_b16 a[150:151], v11 offset:1288\n"
    "buffer_load_dwordx4 v2, s[24:27], s65 offen offset:0 lds\n"
    "v_mfma_f32_16x16x32_bf16 v[96:99], a[160:163], v[220:223], v[96:99]\n"
    "v_mfma_f32_16x16x32_bf16 v[100:103], a[164:167], v[220:223], v[100:103]\n"
    "ds_read_b64_tr_b16 a[152:153], v11 offset:1536\n"
    "v_mfma_f32_16x16x32_bf16 v[104:107], a[168:171], v[220:223], v[104:107]\n"
    "ds_read_b64_tr_b16 a[154:155], v11 offset:1792\n"
    "v_mfma_f32_16x16x32_bf16 v[108:111], a[172:175], v[220:223], v[108:111]\n"
    "ds_read_b64_tr_b16 a[156:157], v11 offset:1544\n"
    "buffer_load_dwordx4 v3, s[24:27], s65 offen offset:1024 lds\n"
    "v_mfma_f32_16x16x32_bf16 v[112:115], a[176:179], v[220:223], v[112:115]\n"
    "ds_read_b64_tr_b16 a[158:159], v11 offset:1800\n"
    "v_mfma_f32_16x16x32_bf16 v[116:119], a[180:183], v[220:223], v[116:119]\n"
    "v_mfma_f32_16x16x32_bf16 v[120:123], a[184:187], v[220:223], v[120:123]\n"
    "ds_read_b128 v[196:199], v12 offset:2048\n"
    "buffer_load_dwordx4 v4, s[24:27], s65 offen offset:2048 lds\n"
    "v_mfma_f32_16x16x32_bf16 v[124:127], a[188:191], v[220:223], v[124:127]\n"
    "ds_read_b128 v[200:203], v12 offset:4096\n"
    "v_mfma_f32_16x16x32_bf16 v[128:131], a[160:163], v[224:227], v[128:131]\n"
    "ds_read_b128 v[204:207], v12 offset:6144\n"
    "v_mfma_f32_16x16x32_bf16 v[132:135], a[164:167], v[224:227], v[132:135]\n"
    "ds_read_b128 v[208:211], v12 offset:8192\n"
    "v_mfma_f32_16x16x32_bf16 v[136:139], a[168:171], v[224:227], v[136:139]\n"
    "buffer_load_dwordx4 v5, s[24:27], s65 offen offset:3072 lds\n"
    "v_mfma_f32_16x16x32_bf16 v[140:143], a[172:175], v[224:227], v[140:143]\n"
    "v_mfma_f32_16x16x32_bf16 v[144:147], a[176:179], v[224:227], v[144:147]\n"
    "v_mfma_f32_16x16x32_bf16 v[148:151], a[180:183], v[224:227], v[148:151]\n"
    "s_add_u32 m0, s74, 110592\n"
    "v_mfma_f32_16x16x32_bf16 v[152:155], a[184:187], v[224:227], v[152:155]\n"
    "v_mfma_f32_16x16x32_bf16 v[156:159], a[188:191], v[224:227], v[156:159]\n"
    "v_mfma_f32_16x16x32_bf16 v[160:163], a[160:163], v[228:231], v[160:163]\n"
    "v_mfma_f32_16x16x32_bf16 v[164:167], a[164:167], v[228:231], v[164:167]\n"
    "s_add_u32 s65, s65, 131072\n"
    "v_mfma_f32_16x16x32_bf16 v[168:171], a[168:171], v[228:231], v[168:171]\n"
    "v_mfma_f32_16x16x32_bf16 v[172:175], a[172:175], v[228:231], v[172:175]\n"
    "v_mfma_f32_16x16x32_bf16 v[176:179], a[176:179], v[228:231], v[176:179]\n"
    "buffer_load_dwordx4 v2, s[24:27], s65 offen offset:0 lds\n"
    "v_mfma_f32_16x16x32_bf16 v[180:183], a[180:183], v[228:231], v[180:183]\n"
    "v_mfma_f32_16x16x32_bf16 v[184:187], a[184:187], v[228:231], v[184:187]\n"
    "v_mfma_f32_16x16x32_bf16 v[188:191], a[188:191], v[228:231], v[188:191]\n"
    "s_waitcnt lgkmcnt(0)\n"
    "v_mfma_f32_16x16x32_bf16 v[32:35], a[128:131], v[192:195], v[32:35]\n"
    "v_mfma_f32_16x16x32_bf16 v[36:39], a[132:135], v[192:195], v[36:39]\n"
    "ds_read_b128 v[212:215], v13 offset:0\n"
    "buffer_load_dwordx4 v3, s[24:27], s65 offen offset:1024 lds\n"
    "v_mfma_f32_16x16x32_bf16 v[40:43], a[136:139], v[192:195], v[40:43]\n"
    "ds_read_b64_tr_b16 a[160:161], v11 offset:8192\n"
    "v_mfma_f32_16x16x32_bf16 v[44:47], a[140:143], v[192:195], v[44:47]\n"
    "ds_read_b64_tr_b16 a[162:163], v11 offset:8448\n"
    "buffer_load_dwordx4 v4, s[24:27], s65 offen offset:2048 lds\n"
    "v_mfma_f32_16x16x32_bf16 v[48:51], a[144:147], v[192:195], v[48:51]\n"
    "ds_read_b64_tr_b16 a[164:165], v11 offset:8200\n"
    "v_mfma_f32_16x16x32_bf16 v[52:55], a[148:151], v[192:195], v[52:55]\n"
    "ds_read_b64_tr_b16 a[166:167], v11 offset:8456\n"
    "v_mfma_f32_16x16x32_bf16 v[56:59], a[152:155], v[192:195], v[56:59]\n"
    "buffer_load_dwordx4 v5, s[24:27], s65 offen offset:3072 lds\n"
    "v_mfma_f32_16x16x32_bf16 v[60:63], a[156:159], v[192:195], v[60:63]\n"
    "ds_read_b64_tr_b16 a[168:169], v11 offset:8704\n"
    "v_mfma_f32_16x16x32_bf16 v[64:67], a[128:131], v[196:199], v[64:67]\n"
    "ds_read_b64_tr_b16 a[170:171], v11 offset:8960\n"
    "v_mfma_f32_16x16x32_bf16 v[68:71], a[132:135], v[196:199], v[68:71]\n"
    "ds_read_b64_tr_b16 a[172:173], v11 offset:8712\n"
    "s_add_u32 m0, s73, 106496\n"
    "v_mfma_f32_16x16x32_bf16 v[72:75], a[136:139], v[196:199], v[72:75]\n"
    "ds_read_b64_tr_b16 a[174:175], v11 offset:8968\n"
    "v_mfma_f32_16x16x32_bf16 v[76:79], a[140:143], v[196:199], v[76:79]\n"
    "v_mfma_f32_16x16x32_bf16 v[80:83], a[144:147], v[196:199], v[80:83]\n"
    "ds_read_b64_tr_b16 a[176:177], v11 offset:9216\n"
    "s_add_u32 s64, s64, s71\n"
    "v_mfma_f32_16x16x32_bf16 v[84:87], a[148:151], v[196:199], v[84:87]\n"
    "ds_read_b64_tr_b16 a[178:179], v11 offset:9472\n"
    "v_mfma_f32_16x16x32_bf16 v[88:91], a[152:155], v[196:199], v[88:91]\n"
    "ds_read_b64_tr_b16 a[180:181], v11 offset:9224\n"
    "v_mfma_f32_16x16x32_bf16 v[92:95], a[156:159], v[196:199], v[92:95]\n"
    "ds_read_b64_tr_b16 a[182:183], v11 offset:9480\n"
    "buffer_load_dwordx4 v6, s[20:23], s64 offen offset:0 lds\n"
    "v_mfma_f32_16x16x32_bf16 v[96:99], a[128:131], v[200:203], v[96:99]\n"
    "v_mfma_f32_16x16x32_bf16 v[100:103], a[132:135], v[200:203], v[100:103]\n"
    "ds_read_b64_tr_b16 a[184:185], v11 offset:9728\n"
    "v_mfma_f32_16x16x32_bf16 v[104:107], a[136:139], v[200:203], v[104:107]\n"
    "ds_read_b64_tr_b16 a[186:187], v11 offset:9984\n"
    "buffer_load_dwordx4 v7, s[20:23], s64 offen offset:1024 lds\n"
    "v_mfma_f32_16x16x32_bf16 v[108:111], a[140:143], v[200:203], v[108:111]\n"
    "ds_read_b64_tr_b16 a[188:189], v11 offset:9736\n"
    "v_mfma_f32_16x16x32_bf16 v[112:115], a[144:147], v[200:203], v[112:115]\n"
    "ds_read_b64_tr_b16 a[190:191], v11 offset:9992\n"
    "buffer_load_dwordx4 v8, s[20:23], s64 offen offset:2048 lds\n"
    "v_mfma_f32_16x16x32_bf16 v[116:119], a[148:151], v[200:203], v[116:119]\n"
    "v_mfma_f32_16x16x32_bf16 v[120:123], a[152:155], v[200:203], v[120:123]\n"
    "ds_read_b128 v[216:219], v13 offset:2048\n"
    "v_mfma_f32_16x16x32_bf16 v[124:127], a[156:159], v[200:203], v[124:127]\n"
    "ds_read_b128 v[220:223], v13 offset:4096\n"
    "buffer_load_dwordx4 v9, s[20:23], s64 offen offset:3072 lds\n"
    "v_mfma_f32_16x16x32_bf16 v[128:131], a[128:131], v[204:207], v[128:131]\n"
    "ds_read_b128 v[224:227], v13 offset:6144\n"
    "v_mfma_f32_16x16x32_bf16 v[132:135], a[132:135], v[204:207], v[132:135]\n"
    "ds_read_b128 v[228:231], v13 offset:8192\n"
    "v_mfma_f32_16x16x32_bf16 v[136:139], a[136:139], v[204:207], v[136:139]\n"
    "s_add_u32 m0, s73, 110592\n"
    "v_mfma_f32_16x16x32_bf16 v[140:143], a[140:143], v[204:207], v[140:143]\n"
    "v_mfma_f32_16x16x32_bf16 v[144:147], a[144:147], v[204:207], v[144:147]\n"
    "v_mfma_f32_16x16x32_bf16 v[148:151], a[148:151], v[204:207], v[148:151]\n"
    "s_add_u32 s70, s70, 1\n"
    "v_mfma_f32_16x16x32_bf16 v[152:155], a[152:155], v[204:207], v[152:155]\n"
    "v_mfma_f32_16x16x32_bf16 v[156:159], a[156:159], v[204:207], v[156:159]\n"
    "v_mfma_f32_16x16x32_bf16 v[160:163], a[128:131], v[208:211], v[160:163]\n"
    "buffer_load_dwordx4 v10, s[20:23], s64 offen offset:0 lds\n"
    "v_mfma_f32_16x16x32_bf16 v[164:167], a[132:135], v[208:211], v[164:167]\n"
    "v_mfma_f32_16x16x32_bf16 v[168:171], a[136:139], v[208:211], v[168:171]\n"
    "v_mfma_f32_16x16x32_bf16 v[172:175], a[140:143], v[208:211], v[172:175]\n"
    "v_mfma_f32_16x16x32_bf16 v[176:179], a[144:147], v[208:211], v[176:179]\n"
    "v_mfma_f32_16x16x32_bf16 v[180:183], a[148:151], v[208:211], v[180:183]\n"
    "v_mfma_f32_16x16x32_bf16 v[184:187], a[152:155], v[208:211], v[184:187]\n"
    "v_mfma_f32_16x16x32_bf16 v[188:191], a[156:159], v[208:211], v[188:191]\n"
    "s_waitcnt lgkmcnt(0)\n"
    "s_waitcnt vmcnt(13)\n"
    "s_barrier\n"
    "s_or_b32 s69, s58, s81\n"
    "s_cmp_lg_u32 s69, 0\n"
    "s_cbranch_scc1 HSR3_SKIP\n"
    "s_mov_b64 exec, 0xff\n"
    "s_waitcnt vmcnt(13)\n"
    "v_cmp_eq_u32 vcc, 1, v238\n"
    "s_nop 2\n"
    "v_cndmask_b32 v239, v239, v236, vcc\n"
    "v_cndmask_b32 v251, v238, 2, vcc\n"
    "v_cmp_eq_u32 s[88:89], s80, v237\n"
    "v_cmp_eq_u32 vcc, 0, v251\n"
    "s_nop 2\n"
    "s_and_b64 vcc, vcc, s[88:89]\n"
    "v_cndmask_b32 v238, v251, 1, vcc\n"
    "global_store_dword v242, v236, s[56:57] sc1\n"
    "v_cmp_eq_u32 vcc, 2, v238\n"
    "s_nop 2\n"
    "s_cmp_eq_u64 vcc, 0xff\n"
    "s_cselect_b32 s81, 1, 0\n"
    "global_load_dword v236, v240, s[56:57] sc1\n"
    "global_load_dword v237, v241, s[56:57] sc1\n"
    "s_mov_b64 exec, -1\n"
    "HSR3_SKIP:\n"
    "v_mfma_f32_16x16x32_bf16 v[32:35], a[160:163], v[212:215], v[32:35]\n"
    "v_add_u32 v11, 0xd000, v11\n"
    "v_add_u32 v12, 0xd000, v12\n"
    "v_add_u32 v13, 0xd000, v13\n"
    "v_mfma_f32_16x16x32_bf16 v[36:39], a[164:167], v[212:215], v[36:39]\n"
    "ds_read_b128 v[192:195], v12 offset:0\n"
    "s_mov_b32 s75, s77\n"
    "v_mfma_f32_16x16x32_bf16 v[40:43], a[168:171], v[212:215], v[40:43]\n"
    "ds_read_b64_tr_b16 a[128:129], v11 offset:0\n"
    "v_mfma_f32_16x16x32_bf16 v[44:47], a[172:175], v[212:215], v[44:47]\n"
    "ds_read_b64_tr_b16 a[130:131], v11 offset:256\n"
    "s_mov_b32 s76, 0\n"
    "v_mfma_f32_16x16x32_bf16 v[48:51], a[176:179], v[212:215], v[48:51]\n"
    "ds_read_b64_tr_b16 a[132:133], v11 offset:8\n"
    "v_mfma_f32_16x16x32_bf16 v[52:55], a[180:183], v[212:215], v[52:55]\n"
    "ds_read_b64_tr_b16 a[134:135], v11 offset:264\n"
    "s_add_u32 m0, s76, s78\n"
    "v_mfma_f32_16x16x32_bf16 v[56:59], a[184:187], v[212:215], v[56:59]\n"
    "v_mfma_f32_16x16x32_bf16 v[60:63], a[188:191], v[212:215], v[60:63]\n"
    "ds_read_b64_tr_b16 a[136:137], v11 offset:512\n"
    "v_mfma_f32_16x16x32_bf16 v[64:67], a[160:163], v[216:219], v[64:67]\n"
    "ds_read_b64_tr_b16 a[138:139], v11 offset:768\n"
    "buffer_load_dwordx4 v14, s[28:31], s75 offen offset:0 nt lds\n"
    "v_mfma_f32_16x16x32_bf16 v[68:71], a[164:167], v[216:219], v[68:71]\n"
    "ds_read_b64_tr_b16 a[140:141], v11 offset:520\n"
    "v_mfma_f32_16x16x32_bf16 v[72:75], a[168:171], v[216:219], v[72:75]\n"
    "ds_read_b64_tr_b16 a[142:143], v11 offset:776\n"
    "buffer_load_dwordx4 v15, s[28:31], s75 offen offset:1024 nt lds\n"
    "v_mfma_f32_16x16x32_bf16 v[76:79], a[172:175], v[216:219], v[76:79]\n"
    "v_mfma_f32_16x16x32_bf16 v[80:83], a[176:179], v[216:219], v[80:83]\n"
    "ds_read_b64_tr_b16 a[144:145], v11 offset:1024\n"
    "v_mfma_f32_16x16x32_bf16 v[84:87], a[180:183], v[216:219], v[84:87]\n"
    "ds_read_b64_tr_b16 a[146:147], v11 offset:1280\n"
    "buffer_load_dwordx4 v16, s[28:31], s75 offen offset:2048 nt lds\n"
    "v_mfma_f32_16x16x32_bf16 v[88:91], a[184:187], v[216:219], v[88:91]\n"
    "ds_read_b64_tr_b16 a[148:149], v11 offset:1032\n"
    "v_mfma_f32_16x16x32_bf16 v[92:95], a[188:191], v[216:219], v[92:95]\n"
    "ds_read_b64_tr_b16 a[150:151], v11 offset:1288\n"
    "buffer_load_dwordx4 v17, s[28:31], s75 offen offset:3072 nt lds\n"
    "v_mfma_f32_16x16x32_bf16 v[96:99], a[160:163], v[220:223], v[96:99]\n"
    "v_mfma_f32_16x16x32_bf16 v[100:103], a[164:167], v[220:223], v[100:103]\n"
    "ds_read_b64_tr_b16 a[152:153], v11 offset:1536\n"
    "v_mfma_f32_16x16x32_bf16 v[104:107], a[168:171], v[220:223], v[104:107]\n"
    "ds_read_b64_tr_b16 a[154:155], v11 offset:1792\n"
    "s_add_u32 s64, s76, s78\n"
    "v_mfma_f32_16x16x32_bf16 v[108:111], a[172:175], v[220:223], v[108:111]\n"
    "ds_read_b64_tr_b16 a[156:157], v11 offset:1544\n"
    "v_mfma_f32_16x16x32_bf16 v[112:115], a[176:179], v[220:223], v[112:115]\n"
    "ds_read_b64_tr_b16 a[158:159], v11 offset:1800\n"
    "s_add_u32 m0, s64, 16384\n"
    "v_mfma_f32_16x16x32_bf16 v[116:119], a[180:183], v[220:223], v[116:119]\n"
    "v_mfma_f32_16x16x32_bf16 v[120:123], a[184:187], v[220:223], v[120:123]\n"
    "ds_read_b128 v[196:199], v12 offset:2048\n"
    "buffer_load_dwordx4 v18, s[36:39], s75 offen offset:0 nt lds\n"
    "v_mfma_f32_16x16x32_bf16 v[124:127], a[188:191], v[220:223], v[124:127]\n"
    "ds_read_b128 v[200:203], v12 offset:4096\n"
    "v_mfma_f32_16x16x32_bf16 v[128:131], a[160:163], v[224:227], v[128:131]\n"
    "ds_read_b128 v[204:207], v12 offset:6144\n"
    "v_mfma_f32_16x16x32_bf16 v[132:135], a[164:167], v[224:227], v[132:135]\n"
    "ds_read_b128 v[208:211], v12 offset:8192\n"
    "buffer_load_dwordx4 v19, s[36:39], s75 offen offset:1024 nt lds\n"
    "v_mfma_f32_16x16x32_bf16 v[136:139], a[168:171], v[224:227], v[136:139]\n"
    "v_mfma_f32_16x16x32_bf16 v[140:143], a[172:175], v[224:227], v[140:143]\n"
    "buffer_load_dwordx4 v18, s[36:39], s75 offen offset:2048 nt lds\n"
    "v_mfma_f32_16x16x32_bf16 v[144:147], a[176:179], v[224:227], v[144:147]\n"
    "v_mfma_f32_16x16x32_bf16 v[148:151], a[180:183], v[224:227], v[148:151]\n"
    "v_mfma_f32_16x16x32_bf16 v[152:155], a[184:187], v[224:227], v[152:155]\n"
    "buffer_load_dwordx4 v19, s[36:39], s75 offen offset:3072 nt lds\n"
    "v_mfma_f32_16x16x32_bf16 v[156:159], a[188:191], v[224:227], v[156:159]\n"
    "v_mfma_f32_16x16x32_bf16 v[160:163], a[160:163], v[228:231], v[160:163]\n"
    "s_add_u32 s75, s75, 8192\n"
    "v_mfma_f32_16x16x32_bf16 v[164:167], a[164:167], v[228:231], v[164:167]\n"
    "v_mfma_f32_16x16x32_bf16 v[168:171], a[168:171], v[228:231], v[168:171]\n"
    "v_mfma_f32_16x16x32_bf16 v[172:175], a[172:175], v[228:231], v[172:175]\n"
    "s_add_u32 s76, s76, 32768\n"
    "v_mfma_f32_16x16x32_bf16 v[176:179], a[176:179], v[228:231], v[176:179]\n"
    "v_mfma_f32_16x16x32_bf16 v[180:183], a[180:183], v[228:231], v[180:183]\n"
    "s_and_b32 s76, s76, 0x1ffff\n"
    "v_mfma_f32_16x16x32_bf16 v[184:187], a[184:187], v[228:231], v[184:187]\n"
    "v_mfma_f32_16x16x32_bf16 v[188:191], a[188:191], v[228:231], v[188:191]\n"
    "s_waitcnt lgkmcnt(0)\n"
    "v_mfma_f32_16x16x32_bf16 v[32:35], a[128:131], v[192:195], v[32:35]\n"
    "v_mfma_f32_16x16x32_bf16 v[36:39], a[132:135], v[192:195], v[36:39]\n"
    "ds_read_b128 v[212:215], v13 offset:0\n"
    "s_add_u32 m0, s76, s78\n"
    "v_mfma_f32_16x16x32_bf16 v[40:43], a[136:139], v[192:195], v[40:43]\n"
    "ds_read_b64_tr_b16 a[160:161], v11 offset:8192\n"
    "v_mfma_f32_16x16x32_bf16 v[44:47], a[140:143], v[192:195], v[44:47]\n"
    "ds_read_b64_tr_b16 a[162:163], v11 offset:8448\n"
    "v_mfma_f32_16x16x32_bf16 v[48:51], a[144:147], v[192:195], v[48:51]\n"
    "ds_read_b64_tr_b16 a[164:165], v11 offset:8200\n"
    "v_mfma_f32_16x16x32_bf16 v[52:55], a[148:151], v[192:195], v[52:55]\n"
    "ds_read_b64_tr_b16 a[166:167], v11 offset:8456\n"
    "v_mfma_f32_16x16x32_bf16 v[56:59], a[152:155], v[192:195], v[56:59]\n"
    "v_mfma_f32_16x16x32_bf16 v[60:63], a[156:159], v[192:195], v[60:63]\n"
    "ds_read_b64_tr_b16 a[168:169], v11 offset:8704\n"
    "v_mfma_f32_16x16x32_bf16 v[64:67], a[128:131], v[196:199], v[64:67]\n"
    "ds_read_b64_tr_b16 a[170:171], v11 offset:8960\n"
    "buffer_load_dwordx4 v14, s[28:31], s75 offen offset:0 nt lds\n"
    "v_mfma_f32_16x16x32_bf16 v[68:71], a[132:135], v[196:199], v[68:71]\n"
    "ds_read_b64_tr_b16 a[172:173], v11 offset:8712\n"
    "v_mfma_f32_16x16x32_bf16 v[72:75], a[136:139], v[196:199], v[72:75]\n"
    "ds_read_b64_tr_b16 a[174:175], v11 offset:8968\n"
    "v_mfma_f32_16x16x32_bf16 v[76:79], a[140:143], v[196:199], v[76:79]\n"
    "v_mfma_f32_16x16x32_bf16 v[80:83], a[144:147], v[196:199], v[80:83]\n"
    "ds_read_b64_tr_b16 a[176:177], v11 offset:9216\n"
    "v_mfma_f32_16x16x32_bf16 v[84:87], a[148:151], v[196:199], v[84:87]\n"
    "ds_read_b64_tr_b16 a[178:179], v11 offset:9472\n"
    "v_mfma_f32_16x16x32_bf16 v[88:91], a[152:155], v[196:199], v[88:91]\n"
    "ds_read_b64_tr_b16 a[180:181], v11 offset:9224\n"
    "v_mfma_f32_16x16x32_bf16 v[92:95], a[156:159], v[196:199], v[92:95]\n"
    "ds_read_b64_tr_b16 a[182:183], v11 offset:9480\n"
    "v_mfma_f32_16x16x32_bf16 v[96:99], a[128:131], v[200:203], v[96:99]\n"
    "buffer_load_dwordx4 v15, s[28:31], s75 offen offset:1024 nt lds\n"
    "v_mfma_f32_16x16x32_bf16 v[100:103], a[132:135], v[200:203], v[100:103]\n"
    "ds_read_b64_tr_b16 a[184:185], v11 offset:9728\n"
    "v_mfma_f32_16x16x32_bf16 v[104:107], a[136:139], v[200:203], v[104:107]\n"
    "ds_read_b64_tr_b16 a[186:187], v11 offset:9984\n"
    "v_mfma_f32_16x16x32_bf16 v[108:111], a[140:143], v[200:203], v[108:111]\n"
    "ds_read_b64_tr_b16 a[188:189], v11 offset:9736\n"
    "v_mfma_f32_16x16x32_bf16 v[112:115], a[144:147], v[200:203], v[112:115]\n"
    "ds_read_b64_tr_b16 a[190:191], v11 offset:9992\n"
    "v_mfma_f32_16x16x32_bf16 v[116:119], a[148:151], v[200:203], v[116:119]\n"
    "v_mfma_f32_16x16x32_bf16 v[120:123], a[152:155], v[200:203], v[120:123]\n"
    "ds_read_b128 v[216:219], v13 offset:2048\n"
    "v_mfma_f32_16x16x32_bf16 v[124:127], a[156:159], v[200:203], v[124:127]\n"
    "ds_read_b128 v[220:223], v13 offset:4096\n"
    "v_mfma_f32_16x16x32_bf16 v[128:131], a[128:131], v[204:207], v[128:131]\n"
    "ds_read_b128 v[224:227], v13 offset:6144\n"
    "buffer_load_dwordx4 v16, s[28:31], s75 offen offset:2048 nt lds\n"
    "v_mfma_f32_16x16x32_bf16 v[132:135], a[132:135], v[204:207], v[132:135]\n"
    "ds_read_b128 v[228:231], v13 offset:8192\n"
    "v_mfma_f32_16x16x32_bf16 v[136:139], a[136:139], v[204:207], v[136:139]\n"
    "v_mfma_f32_16x16x32_bf16 v[140:143], a[140:143], v[204:207], v[140:143]\n"
    "v_mfma_f32_16x16x32_bf16 v[144:147], a[144:147], v[204:207], v[144:147]\n"
    "v_mfma_f32_16x16x32_bf16 v[148:151], a[148:151], v[204:207], v[148:151]\n"
    "v_mfma_f32_16x16x32_bf16 v[152:155], a[152:155], v[204:207], v[152:155]\n"
    "v_mfma_f32_16x16x32_bf16 v[156:159], a[156:159], v[204:207], v[156:159]\n"
    "v_mfma_f32_16x16x32_bf16 v[160:163], a[128:131], v[208:211], v[160:163]\n"
    "buffer_load_dwordx4 v17, s[28:31], s75 offen offset:3072 nt lds\n"
    "v_mfma_f32_16x16x32_bf16 v[164:167], a[132:135], v[208:211], v[164:167]\n"
    "v_mfma_f32_16x16x32_bf16 v[168:171], a[136:139], v[208:211], v[168:171]\n"
    "v_mfma_f32_16x16x32_bf16 v[172:175], a[140:143], v[208:211], v[172:175]\n"
    "v_mfma_f32_16x16x32_bf16 v[176:179], a[144:147], v[208:211], v[176:179]\n"
    "v_mfma_f32_16x16x32_bf16 v[180:183], a[148:151], v[208:211], v[180:183]\n"
    "v_mfma_f32_16x16x32_bf16 v[184:187], a[152:155], v[208:211], v[184:187]\n"
    "v_mfma_f32_16x16x32_bf16 v[188:191], a[156:159], v[208:211], v[188:191]\n"
    "s_waitcnt lgkmcnt(0)\n"
    "s_waitcnt vmcnt(12)\n"
    "s_barrier\n"
    "v_mfma_f32_16x16x32_bf16 v[32:35], a[160:163], v[212:215], v[32:35]\n"
    "v_add_u32 v11, 0xd000, v11\n"
    "v_add_u32 v12, 0xd000, v12\n"
    "v_add_u32 v13, 0xd000, v13\n"
    "v_mfma_f32_16x16x32_bf16 v[36:39], a[164:167], v[212:215], v[36:39]\n"
    "ds_read_b128 v[192:195], v12 offset:0\n"
    "s_add_u32 s64, s76, s78\n"
    "v_mfma_f32_16x16x32_bf16 v[40:43], a[168:171], v[212:215], v[40:43]\n"
    "ds_read_b64_tr_b16 a[128:129], v11 offset:0\n"
    "v_mfma_f32_16x16x32_bf16 v[44:47], a[172:175], v[212:215], v[44:47]\n"
    "ds_read_b64_tr_b16 a[130:131], v11 offset:256\n"
    "s_add_u32 m0, s64, 16384\n"
    "v_mfma_f32_16x16x32_bf16 v[48:51], a[176:179], v[212:215], v[48:51]\n"
    "ds_read_b64_tr_b16 a[132:133], v11 offset:8\n"
    "v_mfma_f32_16x16x32_bf16 v[52:55], a[180:183], v[212:215], v[52:55]\n"
    "ds_read_b64_tr_b16 a[134:135], v11 offset:264\n"
    "v_mfma_f32_16x16x32_bf16 v[56:59], a[184:187], v[212:215], v[56:59]\n"
    "buffer_load_dwordx4 v18, s[36:39], s75 offen offset:0 nt lds\n"
    "v_mfma_f32_16x16x32_bf16 v[60:63], a[188:191], v[212:215], v[60:63]\n"
    "ds_read_b64_tr_b16 a[136:137], v11 offset:512\n"
    "v_mfma_f32_16x16x32_bf16 v[64:67], a[160:163], v[216:219], v[64:67]\n"
    "ds_read_b64_tr_b16 a[138:139], v11 offset:768\n"
    "v_mfma_f32_16x16x32_bf16 v[68:71], a[164:167], v[216:219], v[68:71]\n"
    "ds_read_b64_tr_b16 a[140:141], v11 offset:520\n"
    "buffer_load_dwordx4 v19, s[36:39], s75 offen offset:1024 nt lds\n"
    "v_mfma_f32_16x16x32_bf16 v[72:75], a[168:171], v[216:219], v[72:75]\n"
    "ds_read_b64_tr_b16 a[142:143], v11 offset:776\n"
    "v_mfma_f32_16x16x32_bf16 v[76:79], a[172:175], v[216:219], v[76:79]\n"
    "v_mfma_f32_16x16x32_bf16 v[80:83], a[176:179], v[216:219], v[80:83]\n"
    "ds_read_b64_tr_b16 a[144:145], v11 offset:1024\n"
    "buffer_load_dwordx4 v18, s[36:39], s75 offen offset:2048 nt lds\n"
    "v_mfma_f32_16x16x32_bf16 v[84:87], a[180:183], v[216:219], v[84:87]\n"
    "ds_read_b64_tr_b16 a[146:147], v11 offset:1280\n"
    "v_mfma_f32_16x16x32_bf16 v[88:91], a[184:187], v[216:219], v[88:91]\n"
    "ds_read_b64_tr_b16 a[148:149], v11 offset:1032\n"
    "buffer_load_dwordx4 v19, s[36:39], s75 offen offset:3072 nt lds\n"
    "v_mfma_f32_16x16x32_bf16 v[92:95], a[188:191], v[216:219], v[92:95]\n"
    "ds_read_b64_tr_b16 a[150:151], v11 offset:1288\n"
    "v_mfma_f32_16x16x32_bf16 v[96:99], a[160:163], v[220:223], v[96:99]\n"
    "v_mfma_f32_16x16x32_bf16 v[100:103], a[164:167], v[220:223], v[100:103]\n"
    "ds_read_b64_tr_b16 a[152:153], v11 offset:1536\n"
    "s_add_u32 s75, s75, 8192\n"
    "v_mfma_f32_16x16x32_bf16 v[104:107], a[168:171], v[220:223], v[104:107]\n"
    "ds_read_b64_tr_b16 a[154:155], v11 offset:1792\n"
    "v_mfma_f32_16x16x32_bf16 v[108:111], a[172:175], v[220:223], v[108:111]\n"
    "ds_read_b64_tr_b16 a[156:157], v11 offset:1544\n"
    "v_mfma_f32_16x16x32_bf16 v[112:115], a[176:179], v[220:223], v[112:115]\n"
    "ds_read_b64_tr_b16 a[158:159], v11 offset:1800\n"
    "s_add_u32 s76, s76, 32768\n"
    "v_mfma_f32_16x16x32_bf16 v[116:119], a[180:183], v[220:223], v[116:119]\n"
    "v_mfma_f32_16x16x32_bf16 v[120:123], a[184:187], v[220:223], v[120:123]\n"
    "ds_read_b128 v[196:199], v12 offset:2048\n"
    "v_mfma_f32_16x16x32_bf16 v[124:127], a[188:191], v[220:223], v[124:127]\n"
    "ds_read_b128 v[200:203], v12 offset:4096\n"
    "s_and_b32 s76, s76, 0x1ffff\n"
    "v_mfma_f32_16x16x32_bf16 v[128:131], a[160:163], v[224:227], v[128:131]\n"
    "ds_read_b128 v[204:207], v12 offset:6144\n"
    "v_mfma_f32_16x16x32_bf16 v[132:135], a[164:167], v[224:227], v[132:135]\n"
    "ds_read_b128 v[208:211], v12 offset:8192\n"
    "v_mfma_f32_16x16x32_bf16 v[136:139], a[168:171], v[224:227], v[136:139]\n"
    "s_add_u32 m0, s76, s78\n"
    "v_mfma_f32_16x16x32_bf16 v[140:143], a[172:175], v[224:227], v[140:143]\n"
    "v_mfma_f32_16x16x32_bf16 v[144:147], a[176:179], v[224:227], v[144:147]\n"
    "buffer_load_dwordx4 v14, s[28:31], s75 offen offset:0 nt lds\n"
    "v_mfma_f32_16x16x32_bf16 v[148:151], a[180:183], v[224:227], v[148:151]\n"
    "v_mfma_f32_16x16x32_bf16 v[152:155], a[184:187], v[224:227], v[152:155]\n"
    "v_mfma_f32_16x16x32_bf16 v[156:159], a[188:191], v[224:227], v[156:159]\n"
    "buffer_load_dwordx4 v15, s[28:31], s75 offen offset:1024 nt lds\n"
    "v_mfma_f32_16x16x32_bf16 v[160:163], a[160:163], v[228:231], v[160:163]\n"
    "v_mfma_f32_16x16x32_bf16 v[164:167], a[164:167], v[228:231], v[164:167]\n"
    "v_mfma_f32_16x16x32_bf16 v[168:171], a[168:171], v[228:231], v[168:171]\n"
    "buffer_load_dwordx4 v16, s[28:31], s75 offen offset:2048 nt lds\n"
    "v_mfma_f32_16x16x32_bf16 v[172:175], a[172:175], v[228:231], v[172:175]\n"
    "v_mfma_f32_16x16x32_bf16 v[176:179], a[176:179], v[228:231], v[176:179]\n"
    "v_mfma_f32_16x16x32_bf16 v[180:183], a[180:183], v[228:231], v[180:183]\n"
    "buffer_load_dwordx4 v17, s[28:31], s75 offen offset:3072 nt lds\n"
    "v_mfma_f32_16x16x32_bf16 v[184:187], a[184:187], v[228:231], v[184:187]\n"
    "v_mfma_f32_16x16x32_bf16 v[188:191], a[188:191], v[228:231], v[188:191]\n"
    "s_waitcnt lgkmcnt(0)\n"
    "v_mfma_f32_16x16x32_bf16 v[32:35], a[128:131], v[192:195], v[32:35]\n"
    "v_mfma_f32_16x16x32_bf16 v[36:39], a[132:135], v[192:195], v[36:39]\n"
    "ds_read_b128 v[212:215], v13 offset:0\n"
    "s_add_u32 s64, s76, s78\n"
    "v_mfma_f32_16x16x32_bf16 v[40:43], a[136:139], v[192:195], v[40:43]\n"
    "ds_read_b64_tr_b16 a[160:161], v11 offset:8192\n"
    "v_mfma_f32_16x16x32_bf16 v[44:47], a[140:143], v[192:195], v[44:47]\n"
    "ds_read_b64_tr_b16 a[162:163], v11 offset:8448\n"
    "v_mfma_f32_16x16x32_bf16 v[48:51], a[144:147], v[192:195], v[48:51]\n"
    "ds_read_b64_tr_b16 a[164:165], v11 offset:8200\n"
    "v_mfma_f32_16x16x32_bf16 v[52:55], a[148:151], v[192:195], v[52:55]\n"
    "ds_read_b64_tr_b16 a[166:167], v11 offset:8456\n"
    "s_add_u32 m0, s64, 16384\n"
    "v_mfma_f32_16x16x32_bf16 v[56:59], a[152:155], v[192:195], v[56:59]\n"
    "v_mfma_f32_16x16x32_bf16 v[60:63], a[156:159], v[192:195], v[60:63]\n"
    "ds_read_b64_tr_b16 a[168:169], v11 offset:8704\n"
    "v_mfma_f32_16x16x32_bf16 v[64:67], a[128:131], v[196:199], v[64:67]\n"
    "ds_read_b64_tr_b16 a[170:171], v11 offset:8960\n"
    "v_mfma_f32_16x16x32_bf16 v[68:71], a[132:135], v[196:199], v[68:71]\n"
    "ds_read_b64_tr_b16 a[172:173], v11 offset:8712\n"
    "buffer_load_dwordx4 v18, s[36:39], s75 offen offset:0 nt lds\n"
    "v_mfma_f32_16x16x32_bf16 v[72:75], a[136:139], v[196:199], v[72:75]\n"
    "ds_read_b64_tr_b16 a[174:175], v11 offset:8968\n"
    "v_mfma_f32_16x16x32_bf16 v[76:79], a[140:143], v[196:199], v[76:79]\n"
    "v_mfma_f32_16x16x32_bf16 v[80:83], a[144:147], v[196:199], v[80:83]\n"
    "ds_read_b64_tr_b16 a[176:177], v11 offset:9216\n"
    "v_mfma_f32_16x16x32_bf16 v[84:87], a[148:151], v[196:199], v[84:87]\n"
    "ds_read_b64_tr_b16 a[178:179], v11 offset:9472\n"
    "v_mfma_f32_16x16x32_bf16 v[88:91], a[152:155], v[196:199], v[88:91]\n"
    "ds_read_b64_tr_b16 a[180:181], v11 offset:9224\n"
    "buffer_load_dwordx4 v19, s[36:39], s75 offen offset:1024 nt lds\n"
    "v_mfma_f32_16x16x32_bf16 v[92:95], a[156:159], v[196:199], v[92:95]\n"
    "ds_read_b64_tr_b16 a[182:183], v11 offset:9480\n"
    "v_mfma_f32_16x16x32_bf16 v[96:99], a[128:131], v[200:203], v[96:99]\n"
    "v_mfma_f32_16x16x32_bf16 v[100:103], a[132:135], v[200:203], v[100:103]\n"
    "ds_read_b64_tr_b16 a[184:185], v11 offset:9728\n"
    "v_mfma_f32_16x16x32_bf16 v[104:107], a[136:139], v[200:203], v[104:107]\n"
    "ds_read_b64_tr_b16 a[186:187], v11 offset:9984\n"
    "buffer_load_dwordx4 v18, s[36:39], s75 offen offset:2048 nt lds\n"
    "v_mfma_f32_16x16x32_bf16 v[108:111], a[140:143], v[200:203], v[108:111]\n"
    "ds_read_b64_tr_b16 a[188:189], v11 offset:9736\n"
    "v_mfma_f32_16x16x32_bf16 v[112:115], a[144:147], v[200:203], v[112:115]\n"
    "ds_read_b64_tr_b16 a[190:191], v11 offset:9992\n"
    "v_mfma_f32_16x16x32_bf16 v[116:119], a[148:151], v[200:203], v[116:119]\n"
    "v_mfma_f32_16x16x32_bf16 v[120:123], a[152:155], v[200:203], v[120:123]\n"
    "ds_read_b128 v[216:219], v13 offset:2048\n"
    "buffer_load_dwordx4 v19, s[36:39], s75 offen offset:3072 nt lds\n"
    "v_mfma_f32_16x16x32_bf16 v[124:127], a[156:159], v[200:203], v[124:127]\n"
    "ds_read_b128 v[220:223], v13 offset:4096\n"
    "v_mfma_f32_16x16x32_bf16 v[128:131], a[128:131], v[204:207], v[128:131]\n"
    "ds_read_b128 v[224:227], v13 offset:6144\n"
    "v_mfma_f32_16x16x32_bf16 v[132:135], a[132:135], v[204:207], v[132:135]\n"
    "ds_read_b128 v[228:231], v13 offset:8192\n"
    "v_mfma_f32_16x16x32_bf16 v[136:139], a[136:139], v[204:207], v[136:139]\n"
    "v_mfma_f32_16x16x32_bf16 v[140:143], a[140:143], v[204:207], v[140:143]\n"
    "s_add_u32 s75, s75, 8192\n"
    "v_mfma_f32_16x16x32_bf16 v[144:147], a[144:147], v[204:207], v[144:147]\n"
    "v_mfma_f32_16x16x32_bf16 v[148:151], a[148:151], v[204:207], v[148:151]\n"
    "v_mfma_f32_16x16x32_bf16 v[152:155], a[152:155], v[204:207], v[152:155]\n"
    "v_mfma_f32_16x16x32_bf16 v[156:159], a[156:159], v[204:207], v[156:159]\n"
    "s_add_u32 s76, s76, 32768\n"
    "v_mfma_f32_16x16x32_bf16 v[160:163], a[128:131], v[208:211], v[160:163]\n"
    "v_mfma_f32_16x16x32_bf16 v[164:167], a[132:135], v[208:211], v[164:167]\n"
    "v_mfma_f32_16x16x32_bf16 v[168:171], a[136:139], v[208:211], v[168:171]\n"
    "v_mfma_f32_16x16x32_bf16 v[172:175], a[140:143], v[208:211], v[172:175]\n"
    "s_and_b32 s76, s76, 0x1ffff\n"
    "v_mfma_f32_16x16x32_bf16 v[176:179], a[144:147], v[208:211], v[176:179]\n"
    "v_mfma_f32_16x16x32_bf16 v[180:183], a[148:151], v[208:211], v[180:183]\n"
    "v_mfma_f32_16x16x32_bf16 v[184:187], a[152:155], v[208:211], v[184:187]\n"
    "v_mfma_f32_16x16x32_bf16 v[188:191], a[156:159], v[208:211], v[188:191]\n"
    "s_waitcnt lgkmcnt(0)\n"
    "s_waitcnt vmcnt(16)\n"
    "s_barrier\n"
    "v_mfma_f32_16x16x32_bf16 v[32:35], a[160:163], v[212:215], v[32:35]\n"
    "v_mfma_f32_16x16x32_bf16 v[36:39], a[164:167], v[212:215], v[36:39]\n"
    "ds_read_b128 a[192:195], v21 offset:0\n"
    "v_mfma_f32_16x16x32_bf16 v[40:43], a[168:171], v[212:215], v[40:43]\n"
    "ds_read_b128 a[196:199], v21 offset:4096\n"
    "v_mfma_f32_16x16x32_bf16 v[44:47], a[172:175], v[212:215], v[44:47]\n"
    "ds_read_b128 a[200:203], v22 offset:0\n"
    "v_mfma_f32_16x16x32_bf16 v[48:51], a[176:179], v[212:215], v[48:51]\n"
    "ds_read_b128 a[204:207], v22 offset:4096\n"
    "v_mfma_f32_16x16x32_bf16 v[52:55], a[180:183], v[212:215], v[52:55]\n"
    "v_mfma_f32_16x16x32_bf16 v[56:59], a[184:187], v[212:215], v[56:59]\n"
    "ds_read_b128 a[208:211], v23 offset:0\n"
    "v_mfma_f32_16x16x32_bf16 v[60:63], a[188:191], v[212:215], v[60:63]\n"
    "ds_read_b128 a[212:215], v23 offset:4096\n"
    "v_mfma_f32_16x16x32_bf16 v[64:67], a[160:163], v[216:219], v[64:67]\n"
    "ds_read_b128 a[216:219], v24 offset:0\n"
    "v_mfma_f32_16x16x32_bf16 v[68:71], a[164:167], v[216:219], v[68:71]\n"
    "ds_read_b128 a[220:223], v24 offset:4096\n"
    "v_mfma_f32_16x16x32_bf16 v[72:75], a[168:171], v[216:219], v[72:75]\n"
    "v_mfma_f32_16x16x32_bf16 v[76:79], a[172:175], v[216:219], v[76:79]\n"
    "ds_read_b64_tr_b16 a[224:225], v25 offset:0\n"
    "v_mul_f32 v32, s83, v32\n"
    "v_mul_f32 v33, s83, v33\n"
    "v_mfma_f32_16x16x32_bf16 v[80:83], a[176:179], v[216:219], v[80:83]\n"
    "ds_read_b64_tr_b16 a[226:227], v25 offset:4096\n"
    "v_mul_f32 v34, s83, v34\n"
    "v_mul_f32 v35, s83, v35\n"
    "v_mfma_f32_16x16x32_bf16 v[84:87], a[180:183], v[216:219], v[84:87]\n"
    "ds_read_b64_tr_b16 a[228:229], v26 offset:0\n"
    "v_mul_f32 v36, s83, v36\n"
    "v_mfma_f32_16x16x32_bf16 v[88:91], a[184:187], v[216:219], v[88:91]\n"
    "ds_read_b64_tr_b16 a[230:231], v26 offset:4096\n"
    "v_mul_f32 v37, s83, v37\n"
    "v_mul_f32 v38, s83, v38\n"
    "v_mfma_f32_16x16x32_bf16 v[92:95], a[188:191], v[216:219], v[92:95]\n"
    "v_mul_f32 v39, s83, v39\n"
    "v_cvt_pk_bf16_f32 v32, v32, v33\n"
    "v_mfma_f32_16x16x32_bf16 v[96:99], a[160:163], v[220:223], v[96:99]\n"
    "ds_read_b64_tr_b16 a[232:233], v25 offset:512\n"
    "v_cvt_pk_bf16_f32 v33, v34, v35\n"
    "v_mfma_f32_16x16x32_bf16 v[100:103], a[164:167], v[220:223], v[100:103]\n"
    "ds_read_b64_tr_b16 a[234:235], v25 offset:4608\n"
    "v_cvt_pk_bf16_f32 v34, v36, v37\n"
    "v_cvt_pk_bf16_f32 v35, v38, v39\n"
    "v_mfma_f32_16x16x32_bf16 v[104:107], a[168:171], v[220:223], v[104:107]\n"
    "ds_read_b64_tr_b16 a[236:237], v26 offset:512\n"
    "v_mul_f32 v40, s83, v40\n"
    "v_mul_f32 v41, s83, v41\n"
    "v_mfma_f32_16x16x32_bf16 v[108:111], a[172:175], v[220:223], v[108:111]\n"
    "ds_read_b64_tr_b16 a[238:239], v26 offset:4608\n"
    "v_mul_f32 v42, s83, v42\n"
    "v_mfma_f32_16x16x32_bf16 v[112:115], a[176:179], v[220:223], v[112:115]\n"
    "v_mul_f32 v43, s83, v43\n"
    "v_mul_f32 v44, s83, v44\n"
    "v_mfma_f32_16x16x32_bf16 v[116:119], a[180:183], v[220:223], v[116:119]\n"
    "ds_read_b64_tr_b16 a[240:241], v25 offset:1024\n"
    "v_mul_f32 v45, s83, v45\n"
    "v_mul_f32 v46, s83, v46\n"
    "v_mfma_f32_16x16x32_bf16 v[120:123], a[184:187], v[220:223], v[120:123]\n"
    "ds_read_b64_tr_b16 a[242:243], v25 offset:5120\n"
    "v_mul_f32 v47, s83, v47\n"
    "v_mfma_f32_16x16x32_bf16 v[124:127], a[188:191], v[220:223], v[124:127]\n"
    "ds_read_b64_tr_b16 a[244:245], v26 offset:1024\n"
    "v_cvt_pk_bf16_f32 v36, v40, v41\n"
    "v_cvt_pk_bf16_f32 v37, v42, v43\n"
    "v_mfma_f32_16x16x32_bf16 v[128:131], a[160:163], v[224:227], v[128:131]\n"
    "ds_read_b64_tr_b16 a[246:247], v26 offset:5120\n"
    "v_cvt_pk_bf16_f32 v38, v44, v45\n"
    "v_cvt_pk_bf16_f32 v39, v46, v47\n"
    "v_mfma_f32_16x16x32_bf16 v[132:135], a[164:167], v[224:227], v[132:135]\n"
    "v_mul_f32 v48, s83, v48\n"
    "v_mfma_f32_16x16x32_bf16 v[136:139], a[168:171], v[224:227], v[136:139]\n"
    "ds_read_b64_tr_b16 a[248:249], v25 offset:1536\n"
    "v_mul_f32 v49, s83, v49\n"
    "v_mul_f32 v50, s83, v50\n"
    "v_mfma_f32_16x16x32_bf16 v[140:143], a[172:175], v[224:227], v[140:143]\n"
    "ds_read_b64_tr_b16 a[250:251], v25 offset:5632\n"
    "v_mul_f32 v51, s83, v51\n"
    "v_mul_f32 v52, s83, v52\n"
    "v_mfma_f32_16x16x32_bf16 v[144:147], a[176:179], v[224:227], v[144:147]\n"
    "ds_read_b64_tr_b16 a[252:253], v26 offset:1536\n"
    "v_mul_f32 v53, s83, v53\n"
    "v_mfma_f32_16x16x32_bf16 v[148:151], a[180:183], v[224:227], v[148:151]\n"
    "ds_read_b64_tr_b16 a[254:255], v26 offset:5632\n"
    "v_mul_f32 v54, s83, v54\n"
    "v_mul_f32 v55, s83, v55\n"
    "v_mfma_f32_16x16x32_bf16 v[152:155], a[184:187], v[224:227], v[152:155]\n"
    "v_cvt_pk_bf16_f32 v40, v48, v49\n"
    "v_cvt_pk_bf16_f32 v41, v50, v51\n"
    "v_mfma_f32_16x16x32_bf16 v[156:159], a[188:191], v[224:227], v[156:159]\n"
    "v_cvt_pk_bf16_f32 v42, v52, v53\n"
    "v_mfma_f32_16x16x32_bf16 v[160:163], a[160:163], v[228:231], v[160:163]\n"
    "v_cvt_pk_bf16_f32 v43, v54, v55\n"
    "v_mul_f32 v56, s83, v56\n"
    "v_mfma_f32_16x16x32_bf16 v[164:167], a[164:167], v[228:231], v[164:167]\n"
    "v_mul_f32 v57, s83, v57\n"
    "v_mul_f32 v58, s83, v58\n"
    "v_mfma_f32_16x16x32_bf16 v[168:171], a[168:171], v[228:231], v[168:171]\n"
    "v_mul_f32 v59, s83, v59\n"
    "v_mfma_f32_16x16x32_bf16 v[172:175], a[172:175], v[228:231], v[172:175]\n"
    "v_mul_f32 v60, s83, v60\n"
    "v_mul_f32 v61, s83, v61\n"
    "v_mfma_f32_16x16x32_bf16 v[176:179], a[176:179], v[228:231], v[176:179]\n"
    "v_mul_f32 v62, s83, v62\n"
    "v_mul_f32 v63, s83, v63\n"
    "v_mfma_f32_16x16x32_bf16 v[180:183], a[180:183], v[228:231], v[180:183]\n"
    "v_cvt_pk_bf16_f32 v44, v56, v57\n"
    "v_mfma_f32_16x16x32_bf16 v[184:187], a[184:187], v[228:231], v[184:187]\n"
    "v_cvt_pk_bf16_f32 v45, v58, v59\n"
    "v_cvt_pk_bf16_f32 v46, v60, v61\n"
    "v_mfma_f32_16x16x32_bf16 v[188:191], a[188:191], v[228:231], v[188:191]\n"
    "v_cvt_pk_bf16_f32 v47, v62, v63\n"
    "v_mov_b32 v248, s83\n"
    "v_bfe_u32 v249, v1, 2, 3\n"
    "v_mul_u32_u24 v249, 768, v249\n"
    "v_add_u32 v249, v249, v18\n"
    "v_add_u32 v7, 3072, v249\n"
    "v_add_u32 v8, 2176, v249\n"
    "v_add_u32 v9, 9216, v249\n"
    "v_add_u32 v10, 8320, v249\n"
    "v_mov_b32 v2, 0xff800000\n"
    "v_mov_b32 v3, 0xff800000\n"
    "v_mov_b32 v4, 0xff800000\n"
    "v_mov_b32 v5, 0xff800000\n"
    "v_mov_b32 v6, 0xff800000\n"
    "v_mov_b32 v249, 0x3f803f80\n"
    "v_accvgpr_write_b32 a180, v249\n"
    "v_accvgpr_write_b32 a181, v249\n"
    "v_accvgpr_write_b32 a182, v249\n"
    "v_accvgpr_write_b32 a183, v249\n"
    "v_pk_mul_f32 v[64:65], v[64:65], v[248:249] op_sel_hi:[1,0]\n"
    "v_pk_mul_f32 v[66:67], v[66:67], v[248:249] op_sel_hi:[1,0]\n"
    "v_pk_mul_f32 v[68:69], v[68:69], v[248:249] op_sel_hi:[1,0]\n"
    "v_pk_mul_f32 v[70:71], v[70:71], v[248:249] op_sel_hi:[1,0]\n"
    "v_cvt_pk_bf16_f32 v48, v64, v65\n"
    "v_cvt_pk_bf16_f32 v49, v66, v67\n"
    "v_cvt_pk_bf16_f32 v50, v68, v69\n"
    "v_cvt_pk_bf16_f32 v51, v70, v71\n"
    "v_pk_mul_f32 v[72:73], v[72:73], v[248:249] op_sel_hi:[1,0]\n"
    "v_pk_mul_f32 v[74:75], v[74:75], v[248:249] op_sel_hi:[1,0]\n"
    "v_pk_mul_f32 v[76:77], v[76:77], v[248:249] op_sel_hi:[1,0]\n"
    "v_pk_mul_f32 v[78:79], v[78:79], v[248:249] op_sel_hi:[1,0]\n"
    "v_cvt_pk_bf16_f32 v52, v72, v73\n"
    "v_cvt_pk_bf16_f32 v53, v74, v75\n"
    "v_cvt_pk_bf16_f32 v54, v76, v77\n"
    "v_cvt_pk_bf16_f32 v55, v78, v79\n"
    "v_pk_mul_f32 v[80:81], v[80:81], v[248:249] op_sel_hi:[1,0]\n"
    "v_pk_mul_f32 v[82:83], v[82:83], v[248:249] op_sel_hi:[1,0]\n"
    "v_pk_mul_f32 v[84:85], v[84:85], v[248:249] op_sel_hi:[1,0]\n"
    "v_pk_mul_f32 v[86:87], v[86:87], v[248:249] op_sel_hi:[1,0]\n"
    "v_cvt_pk_bf16_f32 v56, v80, v81\n"
    "v_cvt_pk_bf16_f32 v57, v82, v83\n"
    "v_cvt_pk_bf16_f32 v58, v84, v85\n"
    "v_cvt_pk_bf16_f32 v59, v86, v87\n"
    "v_pk_mul_f32 v[88:89], v[88:89], v[248:249] op_sel_hi:[1,0]\n"
    "v_pk_mul_f32 v[90:91], v[90:91], v[248:249] op_sel_hi:[1,0]\n"
    "v_pk_mul_f32 v[92:93], v[92:93], v[248:249] op_sel_hi:[1,0]\n"
    "v_pk_mul_f32 v[94:95], v[94:95], v[248:249] op_sel_hi:[1,0]\n"
    "v_cvt_pk_bf16_f32 v60, v88, v89\n"
    "v_cvt_pk_bf16_f32 v61, v90, v91\n"
    "v_cvt_pk_bf16_f32 v62, v92, v93\n"
    "v_cvt_pk_bf16_f32 v63, v94, v95\n"
    "v_pk_mul_f32 v[96:97], v[96:97], v[248:249] op_sel_hi:[1,0]\n"
    "v_pk_mul_f32 v[98:99], v[98:99], v[248:249] op_sel_hi:[1,0]\n"
    "v_pk_mul_f32 v[100:101], v[100:101], v[248:249] op_sel_hi:[1,0]\n"
    "v_pk_mul_f32 v[102:103], v[102:103], v[248:249] op_sel_hi:[1,0]\n"
    "v_cvt_pk_bf16_f32 v64, v96, v97\n"
    "v_cvt_pk_bf16_f32 v65, v98, v99\n"
    "v_cvt_pk_bf16_f32 v66, v100, v101\n"
    "v_cvt_pk_bf16_f32 v67, v102, v103\n"
    "v_pk_mul_f32 v[104:105], v[104:105], v[248:249] op_sel_hi:[1,0]\n"
    "v_pk_mul_f32 v[106:107], v[106:107], v[248:249] op_sel_hi:[1,0]\n"
    "v_pk_mul_f32 v[108:109], v[108:109], v[248:249] op_sel_hi:[1,0]\n"
    "v_pk_mul_f32 v[110:111], v[110:111], v[248:249] op_sel_hi:[1,0]\n"
    "v_cvt_pk_bf16_f32 v68, v104, v105\n"
    "v_cvt_pk_bf16_f32 v69, v106, v107\n"
    "v_cvt_pk_bf16_f32 v70, v108, v109\n"
    "v_cvt_pk_bf16_f32 v71, v110, v111\n"
    "v_pk_mul_f32 v[112:113], v[112:113], v[248:249] op_sel_hi:[1,0]\n"
    "v_pk_mul_f32 v[114:115], v[114:115], v[248:249] op_sel_hi:[1,0]\n"
    "v_pk_mul_f32 v[116:117], v[116:117], v[248:249] op_sel_hi:[1,0]\n"
    "v_pk_mul_f32 v[118:119], v[118:119], v[248:249] op_sel_hi:[1,0]\n"
    "v_cvt_pk_bf16_f32 v72, v112, v113\n"
    "v_cvt_pk_bf16_f32 v73, v114, v115\n"
    "v_cvt_pk_bf16_f32 v74, v116, v117\n"
    "v_cvt_pk_bf16_f32 v75, v118, v119\n"
    "v_pk_mul_f32 v[120:121], v[120:121], v[248:249] op_sel_hi:[1,0]\n"
    "v_pk_mul_f32 v[122:123], v[122:123], v[248:249] op_sel_hi:[1,0]\n"
    "v_pk_mul_f32 v[124:125], v[124:125], v[248:249] op_sel_hi:[1,0]\n"
    "v_pk_mul_f32 v[126:127], v[126:127], v[248:249] op_sel_hi:[1,0]\n"
    "v_cvt_pk_bf16_f32 v76, v120, v121\n"
    "v_cvt_pk_bf16_f32 v77, v122, v123\n"
    "v_cvt_pk_bf16_f32 v78, v124, v125\n"
    "v_cvt_pk_bf16_f32 v79, v126, v127\n"
    "v_pk_mul_f32 v[128:129], v[128:129], v[248:249] op_sel_hi:[1,0]\n"
    "v_pk_mul_f32 v[130:131], v[130:131], v[248:249] op_sel_hi:[1,0]\n"
    "v_pk_mul_f32 v[132:133], v[132:133], v[248:249] op_sel_hi:[1,0]\n"
    "v_pk_mul_f32 v[134:135], v[134:135], v[248:249] op_sel_hi:[1,0]\n"
    "v_cvt_pk_bf16_f32 v80, v128, v129\n"
    "v_cvt_pk_bf16_f32 v81, v130, v131\n"
    "v_cvt_pk_bf16_f32 v82, v132, v133\n"
    "v_cvt_pk_bf16_f32 v83, v134, v135\n"
    "v_pk_mul_f32 v[136:137], v[136:137], v[248:249] op_sel_hi:[1,0]\n"
    "v_pk_mul_f32 v[138:139], v[138:139], v[248:249] op_sel_hi:[1,0]\n"
    "v_pk_mul_f32 v[140:141], v[140:141], v[248:249] op_sel_hi:[1,0]\n"
    "v_pk_mul_f32 v[142:143], v[142:143], v[248:249] op_sel_hi:[1,0]\n"
    "v_cvt_pk_bf16_f32 v84, v136, v137\n"
    "v_cvt_pk_bf16_f32 v85, v138, v139\n"
    "v_cvt_pk_bf16_f32 v86, v140, v141\n"
    "v_cvt_pk_bf16_f32 v87, v142, v143\n"
    "v_pk_mul_f32 v[144:145], v[144:145], v[248:249] op_sel_hi:[1,0]\n"
    "v_pk_mul_f32 v[146:147], v[146:147], v[248:249] op_sel_hi:[1,0]\n"
    "v_pk_mul_f32 v[148:149], v[148:149], v[248:249] op_sel_hi:[1,0]\n"
    "v_pk_mul_f32 v[150:151], v[150:151], v[248:249] op_sel_hi:[1,0]\n"
    "v_cvt_pk_bf16_f32 v88, v144, v145\n"
    "v_cvt_pk_bf16_f32 v89, v146, v147\n"
    "v_cvt_pk_bf16_f32 v90, v148, v149\n"
    "v_cvt_pk_bf16_f32 v91, v150, v151\n"
    "v_pk_mul_f32 v[152:153], v[152:153], v[248:249] op_sel_hi:[1,0]\n"
    "v_pk_mul_f32 v[154:155], v[154:155], v[248:249] op_sel_hi:[1,0]\n"
    "v_pk_mul_f32 v[156:157], v[156:157], v[248:249] op_sel_hi:[1,0]\n"
    "v_pk_mul_f32 v[158:159], v[158:159], v[248:249] op_sel_hi:[1,0]\n"
    "v_cvt_pk_bf16_f32 v92, v152, v153\n"
    "v_cvt_pk_bf16_f32 v93, v154, v155\n"
    "v_cvt_pk_bf16_f32 v94, v156, v157\n"
    "v_cvt_pk_bf16_f32 v95, v158, v159\n"
    "v_pk_mul_f32 v[160:161], v[160:161], v[248:249] op_sel_hi:[1,0]\n"
    "v_pk_mul_f32 v[162:163], v[162:163], v[248:249] op_sel_hi:[1,0]\n"
    "v_pk_mul_f32 v[164:165], v[164:165], v[248:249] op_sel_hi:[1,0]\n"
    "v_pk_mul_f32 v[166:167], v[166:167], v[248:249] op_sel_hi:[1,0]\n"
    "v_cvt_pk_bf16_f32 v96, v160, v161\n"
    "v_cvt_pk_bf16_f32 v97, v162, v163\n"
    "v_cvt_pk_bf16_f32 v98, v164, v165\n"
    "v_cvt_pk_bf16_f32 v99, v166, v167\n"
    "v_pk_mul_f32 v[168:169], v[168:169], v[248:249] op_sel_hi:[1,0]\n"
    "v_pk_mul_f32 v[170:171], v[170:171], v[248:249] op_sel_hi:[1,0]\n"
    "v_pk_mul_f32 v[172:173], v[172:173], v[248:249] op_sel_hi:[1,0]\n"
    "v_pk_mul_f32 v[174:175], v[174:175], v[248:249] op_sel_hi:[1,0]\n"
    "v_cvt_pk_bf16_f32 v100, v168, v169\n"
    "v_cvt_pk_bf16_f32 v101, v170, v171\n"
    "v_cvt_pk_bf16_f32 v102, v172, v173\n"
    "v_cvt_pk_bf16_f32 v103, v174, v175\n"
    "v_pk_mul_f32 v[176:177], v[176:177], v[248:249] op_sel_hi:[1,0]\n"
    "v_pk_mul_f32 v[178:179], v[178:179], v[248:249] op_sel_hi:[1,0]\n"
    "v_pk_mul_f32 v[180:181], v[180:181], v[248:249] op_sel_hi:[1,0]\n"
    "v_pk_mul_f32 v[182:183], v[182:183], v[248:249] op_sel_hi:[1,0]\n"
    "v_cvt_pk_bf16_f32 v104, v176, v177\n"
    "v_cvt_pk_bf16_f32 v105, v178, v179\n"
    "v_cvt_pk_bf16_f32 v106, v180, v181\n"
    "v_cvt_pk_bf16_f32 v107, v182, v183\n"
    "v_pk_mul_f32 v[184:185], v[184:185], v[248:249] op_sel_hi:[1,0]\n"
    "v_pk_mul_f32 v[186:187], v[186:187], v[248:249] op_sel_hi:[1,0]\n"
    "v_pk_mul_f32 v[188:189], v[188:189], v[248:249] op_sel_hi:[1,0]\n"
    "v_pk_mul_f32 v[190:191], v[190:191], v[248:249] op_sel_hi:[1,0]\n"
    "v_cvt_pk_bf16_f32 v108, v184, v185\n"
    "v_cvt_pk_bf16_f32 v109, v186, v187\n"
    "v_cvt_pk_bf16_f32 v110, v188, v189\n"
    "v_cvt_pk_bf16_f32 v111, v190, v191\n"
    "ATT_START:\n"
    "s_waitcnt lgkmcnt(15)\n"
    "v_mfma_f32_16x16x32_bf16 v[112:115], a[192:195], v[32:35], 0\n"
    "v_mfma_f32_16x16x32_bf16 v[116:119], a[196:199], v[32:35], 0\n"
    "s_add_u32 m0, s76, s78\n"
    "v_mfma_f32_16x16x32_bf16 v[112:115], a[200:203], v[36:39], v[112:115]\n"
    "v_mfma_f32_16x16x32_bf16 v[116:119], a[204:207], v[36:39], v[116:119]\n"
    "buffer_load_dwordx4 v14, s[28:31], s75 offen offset:0 nt lds\n"
    "v_mfma_f32_16x16x32_bf16 v[112:115], a[208:211], v[40:43], v[112:115]\n"
    "v_mfma_f32_16x16x32_bf16 v[116:119], a[212:215], v[40:43], v[116:119]\n"
    "buffer_load_dwordx4 v15, s[28:31], s75 offen offset:1024 nt lds\n"
    "v_mfma_f32_16x16x32_bf16 v[112:115], a[216:219], v[44:47], v[112:115]\n"
    "v_mfma_f32_16x16x32_bf16 v[116:119], a[220:223], v[44:47], v[116:119]\n"
    "buffer_load_dwordx4 v16, s[28:31], s75 offen offset:2048 nt lds\n"
    "v_accvgpr_write_b32 a96, 0\n"
    "v_accvgpr_write_b32 a97, 0\n"
    "s_nop 1\n"
    "v_max3_f32 v244, v112, v113, v114\n"
    "s_nop 1\n"
    "v_max3_f32 v244, v244, v115, v116\n"
    "v_max3_f32 v244, v244, v117, v118\n"
    "v_max3_f32 v244, v244, v119, v2\n"
    "v_accvgpr_write_b32 a98, 0\n"
    "v_mov_b32 v245, v244\n"
    "v_mov_b32 v246, v244\n"
    "s_nop 1\n"
    "v_permlane16_swap_b32 v245, v246\n"
    "v_max_f32 v244, v245, v246\n"
    "buffer_load_dwordx4 v17, s[28:31], s75 offen offset:3072 nt lds\n"
    "v_mov_b32 v245, v244\n"
    "v_mov_b32 v246, v244\n"
    "s_nop 1\n"
    "v_permlane32_swap_b32 v245, v246\n"
    "v_max_f32 v244, v245, v246\n"
    "v_accvgpr_write_b32 a99, 0\n"
    "v_sub_f32 v112, v112, v244\n"
    "v_sub_f32 v113, v113, v244\n"
    "v_sub_f32 v114, v114, v244\n"
    "v_sub_f32 v115, v115, v244\n"
    "v_sub_f32 v116, v116, v244\n"
    "v_sub_f32 v117, v117, v244\n"
    "v_sub_f32 v118, v118, v244\n"
    "v_sub_f32 v119, v119, v244\n"
    "v_sub_f32 v172, 0, v244\n"
    "v_sub_f32 v173, 0, v244\n"
    "v_sub_f32 v174, 0, v244\n"
    "v_sub_f32 v175, 0, v244\n"
    "v_exp_f32 v112, v112\n"
    "v_exp_f32 v113, v113\n"
    "s_nop 0\n"
    "v_cvt_pk_bf16_f32 v152, v112, v113\n"
    "s_add_u32 s64, s63, s59\n"
    "v_accvgpr_write_b32 a100, 0\n"
    "v_accvgpr_write_b32 a101, 0\n"
    "v_accvgpr_write_b32 a102, 0\n"
    "v_accvgpr_write_b32 a103, 0\n"
    "v_accvgpr_write_b32 a104, 0\n"
    "v_accvgpr_write_b32 a105, 0\n"
    "v_accvgpr_write_b32 a106, 0\n"
    "v_accvgpr_write_b32 a107, 0\n"
    "v_accvgpr_write_b32 a108, 0\n"
    "v_accvgpr_write_b32 a109, 0\n"
    "v_accvgpr_write_b32 a110, 0\n"
    "v_accvgpr_write_b32 a111, 0\n"
    "v_accvgpr_write_b32 a112, 0\n"
    "v_accvgpr_write_b32 a113, 0\n"
    "v_accvgpr_write_b32 a114, 0\n"
    "v_accvgpr_write_b32 a115, 0\n"
    "v_accvgpr_write_b32 a116, 0\n"
    "v_accvgpr_write_b32 a117, 0\n"
    "v_accvgpr_write_b32 a118, 0\n"
    "v_accvgpr_write_b32 a119, 0\n"
    "v_accvgpr_write_b32 a120, 0\n"
    "v_accvgpr_write_b32 a121, 0\n"
    "v_accvgpr_write_b32 a122, 0\n"
    "v_accvgpr_write_b32 a123, 0\n"
    "v_accvgpr_write_b32 a124, 0\n"
    "v_accvgpr_write_b32 a125, 0\n"
    "v_accvgpr_write_b32 a126, 0\n"
    "v_accvgpr_write_b32 a127, 0\n"
    "v_accvgpr_write_b32 a128, 0\n"
    "v_accvgpr_write_b32 a129, 0\n"
    "v_accvgpr_write_b32 a130, 0\n"
    "v_accvgpr_write_b32 a131, 0\n"
    "v_mfma_f32_16x16x32_bf16 v[120:123], a[192:195], v[48:51], 0\n"
    "v_exp_f32 v114, v114\n"
    "v_mfma_f32_16x16x32_bf16 v[124:127], a[196:199], v[48:51], 0\n"
    "v_exp_f32 v115, v115\n"
    "v_mfma_f32_16x16x32_bf16 v[120:123], a[200:203], v[52:55], v[120:123]\n"
    "v_cvt_pk_bf16_f32 v153, v114, v115\n"
    "s_xor_b32 s64, s64, 0\n"
    "v_mfma_f32_16x16x32_bf16 v[124:127], a[204:207], v[52:55], v[124:127]\n"
    "v_exp_f32 v116, v116\n"
    "v_mfma_f32_16x16x32_bf16 v[120:123], a[208:211], v[56:59], v[120:123]\n"
    "v_exp_f32 v117, v117\n"
    "v_mfma_f32_16x16x32_bf16 v[124:127], a[212:215], v[56:59], v[124:127]\n"
    "v_cvt_pk_bf16_f32 v154, v116, v117\n"
    "s_lshl_b32 s64, s64, 17\n"
    "v_mfma_f32_16x16x32_bf16 v[120:123], a[216:219], v[60:63], v[120:123]\n"
    "v_exp_f32 v118, v118\n"
    "v_mfma_f32_16x16x32_bf16 v[124:127], a[220:223], v[60:63], v[124:127]\n"
    "v_exp_f32 v119, v119\n"
    "s_nop 0\n"
    "v_cvt_pk_bf16_f32 v155, v118, v119\n"
    "s_add_u32 s64, s64, s86\n"
    "s_add_u32 s64, s64, 0\n"
    "s_add_u32 m0, s78, 131072\n"
    "v_accvgpr_write_b32 a132, 0\n"
    "v_accvgpr_write_b32 a133, 0\n"
    "v_max3_f32 v244, v120, v121, v122\n"
    "v_max3_f32 v244, v244, v123, v124\n"
    "v_max3_f32 v244, v244, v125, v126\n"
    "v_max3_f32 v244, v244, v127, v3\n"
    "buffer_load_dwordx4 v7, s[40:43], s64 offen offset:0 lds\n"
    "v_mov_b32 v245, v244\n"
    "v_mov_b32 v246, v244\n"
    "s_nop 1\n"
    "v_permlane16_swap_b32 v245, v246\n"
    "v_max_f32 v244, v245, v246\n"
    "v_accvgpr_write_b32 a134, 0\n"
    "v_mov_b32 v245, v244\n"
    "v_mov_b32 v246, v244\n"
    "s_nop 1\n"
    "v_permlane32_swap_b32 v245, v246\n"
    "v_max_f32 v244, v245, v246\n"
    "v_accvgpr_write_b32 a135, 0\n"
    "v_sub_f32 v120, v120, v244\n"
    "v_sub_f32 v121, v121, v244\n"
    "v_sub_f32 v122, v122, v244\n"
    "v_sub_f32 v123, v123, v244\n"
    "v_accvgpr_write_b32 a136, 0\n"
    "v_sub_f32 v124, v124, v244\n"
    "v_sub_f32 v125, v125, v244\n"
    "v_sub_f32 v126, v126, v244\n"
    "v_sub_f32 v127, v127, v244\n"
    "v_accvgpr_write_b32 a137, 0\n"
    "v_sub_f32 v176, 0, v244\n"
    "v_sub_f32 v177, 0, v244\n"
    "v_sub_f32 v178, 0, v244\n"
    "v_sub_f32 v179, 0, v244\n"
    "v_accvgpr_write_b32 a138, 0\n"
    "v_exp_f32 v120, v120\n"
    "v_exp_f32 v121, v121\n"
    "v_accvgpr_write_b32 a139, 0\n"
    "v_cvt_pk_bf16_f32 v156, v120, v121\n"
    "buffer_load_dwordx4 v8, s[40:43], s64 offen offset:1024 lds\n"
    "v_accvgpr_write_b32 a140, 0\n"
    "v_accvgpr_write_b32 a141, 0\n"
    "v_accvgpr_write_b32 a142, 0\n"
    "v_accvgpr_write_b32 a143, 0\n"
    "v_accvgpr_write_b32 a144, 0\n"
    "v_accvgpr_write_b32 a145, 0\n"
    "v_accvgpr_write_b32 a146, 0\n"
    "v_accvgpr_write_b32 a147, 0\n"
    "v_accvgpr_write_b32 a148, 0\n"
    "v_accvgpr_write_b32 a149, 0\n"
    "v_accvgpr_write_b32 a150, 0\n"
    "v_accvgpr_write_b32 a151, 0\n"
    "v_accvgpr_write_b32 a152, 0\n"
    "v_accvgpr_write_b32 a153, 0\n"
    "v_accvgpr_write_b32 a154, 0\n"
    "v_accvgpr_write_b32 a155, 0\n"
    "v_accvgpr_write_b32 a156, 0\n"
    "v_accvgpr_write_b32 a157, 0\n"
    "v_accvgpr_write_b32 a158, 0\n"
    "v_accvgpr_write_b32 a159, 0\n"
    "v_accvgpr_write_b32 a172, 0\n"
    "v_accvgpr_write_b32 a173, 0\n"
    "v_accvgpr_write_b32 a174, 0\n"
    "v_accvgpr_write_b32 a175, 0\n"
    "v_accvgpr_write_b32 a176, 0\n"
    "v_accvgpr_write_b32 a177, 0\n"
    "v_accvgpr_write_b32 a178, 0\n"
    "v_accvgpr_write_b32 a179, 0\n"
    "v_mfma_f32_16x16x32_bf16 v[128:131], a[192:195], v[64:67], 0\n"
    "v_exp_f32 v122, v122\n"
    "buffer_load_dwordx4 v9, s[40:43], s64 offen offset:2048 lds\n"
    "buffer_load_dwordx4 v10, s[40:43], s64 offen offset:3072 lds\n"
    "v_mfma_f32_16x16x32_bf16 v[132:135], a[196:199], v[64:67], 0\n"
    "v_exp_f32 v123, v123\n"
    "v_mfma_f32_16x16x32_bf16 v[128:131], a[200:203], v[68:71], v[128:131]\n"
    "v_cvt_pk_bf16_f32 v157, v122, v123\n"
    "s_add_u32 s64, s76, s78\n"
    "v_mfma_f32_16x16x32_bf16 v[132:135], a[204:207], v[68:71], v[132:135]\n"
    "v_exp_f32 v124, v124\n"
    "v_mfma_f32_16x16x32_bf16 v[128:131], a[208:211], v[72:75], v[128:131]\n"
    "v_exp_f32 v125, v125\n"
    "v_mfma_f32_16x16x32_bf16 v[132:135], a[212:215], v[72:75], v[132:135]\n"
    "v_cvt_pk_bf16_f32 v158, v124, v125\n"
    "s_add_u32 m0, s64, 16384\n"
    "v_mfma_f32_16x16x32_bf16 v[128:131], a[216:219], v[76:79], v[128:131]\n"
    "v_exp_f32 v126, v126\n"
    "v_mfma_f32_16x16x32_bf16 v[132:135], a[220:223], v[76:79], v[132:135]\n"
    "v_exp_f32 v127, v127\n"
    "s_nop 0\n"
    "v_cvt_pk_bf16_f32 v159, v126, v127\n"
    "s_waitcnt lgkmcnt(0)\n"
    "v_mfma_f32_16x16x32_bf16 a[0:3], a[224:227], v[152:155], 0\n"
    "buffer_load_dwordx4 v18, s[36:39], s75 offen offset:0 nt lds\n"
    "buffer_load_dwordx4 v19, s[36:39], s75 offen offset:1024 nt lds\n"
    "v_mfma_f32_16x16x32_bf16 a[4:7], a[228:231], v[152:155], 0\n"
    "v_max3_f32 v244, v128, v129, v130\n"
    "v_max3_f32 v244, v244, v131, v132\n"
    "v_max3_f32 v244, v244, v133, v134\n"
    "v_max3_f32 v244, v244, v135, v4\n"
    "buffer_load_dwordx4 v18, s[36:39], s75 offen offset:2048 nt lds\n"
    "v_mfma_f32_16x16x32_bf16 a[8:11], a[232:235], v[152:155], 0\n"
    "v_mov_b32 v245, v244\n"
    "v_mov_b32 v246, v244\n"
    "s_nop 1\n"
    "v_permlane16_swap_b32 v245, v246\n"
    "v_max_f32 v244, v245, v246\n"
    "v_mfma_f32_16x16x32_bf16 a[12:15], a[236:239], v[152:155], 0\n"
    "v_mov_b32 v245, v244\n"
    "v_mov_b32 v246, v244\n"
    "s_nop 1\n"
    "v_permlane32_swap_b32 v245, v246\n"
    "v_max_f32 v244, v245, v246\n"
    "v_mfma_f32_16x16x32_bf16 a[16:19], a[240:243], v[152:155], 0\n"
    "v_sub_f32 v128, v128, v244\n"
    "v_sub_f32 v129, v129, v244\n"
    "v_sub_f32 v130, v130, v244\n"
    "v_sub_f32 v131, v131, v244\n"
    "v_mfma_f32_16x16x32_bf16 a[20:23], a[244:247], v[152:155], 0\n"
    "v_sub_f32 v132, v132, v244\n"
    "v_sub_f32 v133, v133, v244\n"
    "v_sub_f32 v134, v134, v244\n"
    "v_sub_f32 v135, v135, v244\n"
    "v_mfma_f32_16x16x32_bf16 a[24:27], a[248:251], v[152:155], 0\n"
    "v_sub_f32 v180, 0, v244\n"
    "v_sub_f32 v181, 0, v244\n"
    "v_sub_f32 v182, 0, v244\n"
    "v_sub_f32 v183, 0, v244\n"
    "v_mfma_f32_16x16x32_bf16 a[28:31], a[252:255], v[152:155], 0\n"
    "v_exp_f32 v128, v128\n"
    "v_exp_f32 v129, v129\n"
    "v_mfma_f32_16x16x32_bf16 a[160:163], a[180:183], v[152:155], 0\n"
    "v_cvt_pk_bf16_f32 v160, v128, v129\n"
    "buffer_load_dwordx4 v19, s[36:39], s75 offen offset:3072 nt lds\n"
    "v_mfma_f32_16x16x32_bf16 v[136:139], a[192:195], v[80:83], 0\n"
    "v_exp_f32 v130, v130\n"
    "s_add_u32 s75, s75, 8192\n"
    "v_mfma_f32_16x16x32_bf16 v[140:143], a[196:199], v[80:83], 0\n"
    "v_exp_f32 v131, v131\n"
    "s_add_u32 s76, s76, 32768\n"
    "s_and_b32 s76, s76, 0x1ffff\n"
    "v_mfma_f32_16x16x32_bf16 v[136:139], a[200:203], v[84:87], v[136:139]\n"
    "v_cvt_pk_bf16_f32 v161, v130, v131\n"
    "v_mfma_f32_16x16x32_bf16 v[140:143], a[204:207], v[84:87], v[140:143]\n"
    "v_exp_f32 v132, v132\n"
    "v_mfma_f32_16x16x32_bf16 v[136:139], a[208:211], v[88:91], v[136:139]\n"
    "v_exp_f32 v133, v133\n"
    "v_mfma_f32_16x16x32_bf16 v[140:143], a[212:215], v[88:91], v[140:143]\n"
    "v_cvt_pk_bf16_f32 v162, v132, v133\n"
    "s_add_u32 s64, s63, s59\n"
    "v_mfma_f32_16x16x32_bf16 v[136:139], a[216:219], v[92:95], v[136:139]\n"
    "v_exp_f32 v134, v134\n"
    "v_mfma_f32_16x16x32_bf16 v[140:143], a[220:223], v[92:95], v[140:143]\n"
    "v_exp_f32 v135, v135\n"
    "s_nop 0\n"
    "v_cvt_pk_bf16_f32 v163, v134, v135\n"
    "v_mfma_f32_16x16x32_bf16 a[32:35], a[224:227], v[156:159], 0\n"
    "s_xor_b32 s64, s64, 0\n"
    "s_lshl_b32 s64, s64, 17\n"
    "s_add_u32 s64, s64, s86\n"
    "s_add_u32 s64, s64, 32768\n"
    "v_mfma_f32_16x16x32_bf16 a[36:39], a[228:231], v[156:159], 0\n"
    "v_max3_f32 v244, v136, v137, v138\n"
    "v_max3_f32 v244, v244, v139, v140\n"
    "v_max3_f32 v244, v244, v141, v142\n"
    "v_max3_f32 v244, v244, v143, v5\n"
    "v_mfma_f32_16x16x32_bf16 a[40:43], a[232:235], v[156:159], 0\n"
    "v_mov_b32 v245, v244\n"
    "v_mov_b32 v246, v244\n"
    "s_nop 1\n"
    "v_permlane16_swap_b32 v245, v246\n"
    "v_max_f32 v244, v245, v246\n"
    "s_add_u32 m0, s78, 147456\n"
    "v_mfma_f32_16x16x32_bf16 a[44:47], a[236:239], v[156:159], 0\n"
    "v_mov_b32 v245, v244\n"
    "v_mov_b32 v246, v244\n"
    "s_nop 1\n"
    "v_permlane32_swap_b32 v245, v246\n"
    "v_max_f32 v244, v245, v246\n"
    "v_mfma_f32_16x16x32_bf16 a[48:51], a[240:243], v[156:159], 0\n"
    "v_sub_f32 v136, v136, v244\n"
    "v_sub_f32 v137, v137, v244\n"
    "v_sub_f32 v138, v138, v244\n"
    "v_sub_f32 v139, v139, v244\n"
    "v_mfma_f32_16x16x32_bf16 a[52:55], a[244:247], v[156:159], 0\n"
    "v_sub_f32 v140, v140, v244\n"
    "v_sub_f32 v141, v141, v244\n"
    "v_sub_f32 v142, v142, v244\n"
    "v_sub_f32 v143, v143, v244\n"
    "v_mfma_f32_16x16x32_bf16 a[56:59], a[248:251], v[156:159], 0\n"
    "v_sub_f32 v184, 0, v244\n"
    "v_sub_f32 v185, 0, v244\n"
    "v_sub_f32 v186, 0, v244\n"
    "v_sub_f32 v187, 0, v244\n"
    "v_mfma_f32_16x16x32_bf16 a[60:63], a[252:255], v[156:159], 0\n"
    "v_exp_f32 v136, v136\n"
    "v_exp_f32 v137, v137\n"
    "v_mfma_f32_16x16x32_bf16 a[164:167], a[180:183], v[156:159], 0\n"
    "v_cvt_pk_bf16_f32 v164, v136, v137\n"
    "buffer_load_dwordx4 v7, s[40:43], s64 offen offset:0 lds\n"
    "buffer_load_dwordx4 v8, s[40:43], s64 offen offset:1024 lds\n"
    "buffer_load_dwordx4 v9, s[40:43], s64 offen offset:2048 lds\n"
    "buffer_load_dwordx4 v10, s[40:43], s64 offen offset:3072 lds\n"
    "s_waitcnt lgkmcnt(0)\n"
    "s_waitcnt vmcnt(24)\n"
    "s_barrier\n"
    "v_mfma_f32_16x16x32_bf16 v[144:147], a[192:195], v[96:99], 0\n"
    "v_exp_f32 v138, v138\n"
    "v_mfma_f32_16x16x32_bf16 v[148:151], a[196:199], v[96:99], 0\n"
    "v_exp_f32 v139, v139\n"
    "v_mfma_f32_16x16x32_bf16 v[144:147], a[200:203], v[100:103], v[144:147]\n"
    "v_cvt_pk_bf16_f32 v165, v138, v139\n"
    "ds_read_b128 a[192:195], v21 offset:32768\n"
    "v_mfma_f32_16x16x32_bf16 v[148:151], a[204:207], v[100:103], v[148:151]\n"
    "v_exp_f32 v140, v140\n"
    "ds_read_b128 a[200:203], v22 offset:32768\n"
    "v_mfma_f32_16x16x32_bf16 v[144:147], a[208:211], v[104:107], v[144:147]\n"
    "v_exp_f32 v141, v141\n"
    "ds_read_b128 a[204:207], v22 offset:36864\n"
    "v_mfma_f32_16x16x32_bf16 v[148:151], a[212:215], v[104:107], v[148:151]\n"
    "v_cvt_pk_bf16_f32 v166, v140, v141\n"
    "ds_read_b128 a[196:199], v21 offset:36864\n"
    "v_mfma_f32_16x16x32_bf16 v[144:147], a[216:219], v[108:111], v[144:147]\n"
    "v_exp_f32 v142, v142\n"
    "v_mfma_f32_16x16x32_bf16 v[148:151], a[220:223], v[108:111], v[148:151]\n"
    "v_exp_f32 v143, v143\n"
    "s_nop 0\n"
    "v_cvt_pk_bf16_f32 v167, v142, v143\n"
    "v_mfma_f32_16x16x32_bf16 a[64:67], a[224:227], v[160:163], 0\n"
    "ds_read_b128 a[208:211], v23 offset:32768\n"
    "ds_read_b128 a[212:215], v23 offset:36864\n"
    "ds_read_b128 a[216:219], v24 offset:32768\n"
    "ds_read_b128 a[220:223], v24 offset:36864\n"
    "v_mfma_f32_16x16x32_bf16 a[68:71], a[228:231], v[160:163], 0\n"
    "v_max3_f32 v244, v144, v145, v146\n"
    "v_max3_f32 v244, v244, v147, v148\n"
    "v_max3_f32 v244, v244, v149, v150\n"
    "v_max3_f32 v244, v244, v151, v6\n"
    "v_mfma_f32_16x16x32_bf16 a[72:75], a[232:235], v[160:163], 0\n"
    "v_mov_b32 v245, v244\n"
    "v_mov_b32 v246, v244\n"
    "s_nop 1\n"
    "v_permlane16_swap_b32 v245, v246\n"
    "v_max_f32 v244, v245, v246\n"
    "v_mfma_f32_16x16x32_bf16 a[76:79], a[236:239], v[160:163], 0\n"
    "v_mov_b32 v245, v244\n"
    "v_mov_b32 v246, v244\n"
    "s_nop 1\n"
    "v_permlane32_swap_b32 v245, v246\n"
    "v_max_f32 v244, v245, v246\n"
    "v_mfma_f32_16x16x32_bf16 a[80:83], a[240:243], v[160:163], 0\n"
    "v_sub_f32 v144, v144, v244\n"
    "v_sub_f32 v145, v145, v244\n"
    "v_sub_f32 v146, v146, v244\n"
    "v_sub_f32 v147, v147, v244\n"
    "v_mfma_f32_16x16x32_bf16 a[84:87], a[244:247], v[160:163], 0\n"
    "v_sub_f32 v148, v148, v244\n"
    "v_sub_f32 v149, v149, v244\n"
    "v_sub_f32 v150, v150, v244\n"
    "v_sub_f32 v151, v151, v244\n"
    "v_mfma_f32_16x16x32_bf16 a[88:91], a[248:251], v[160:163], 0\n"
    "v_sub_f32 v188, 0, v244\n"
    "v_sub_f32 v189, 0, v244\n"
    "v_sub_f32 v190, 0, v244\n"
    "v_sub_f32 v191, 0, v244\n"
    "v_mfma_f32_16x16x32_bf16 a[92:95], a[252:255], v[160:163], 0\n"
    "v_exp_f32 v144, v144\n"
    "v_exp_f32 v145, v145\n"
    "s_nop 0\n"
    "v_cvt_pk_bf16_f32 v168, v144, v145\n"
    "v_mfma_f32_16x16x32_bf16 a[168:171], a[180:183], v[160:163], 0\n"
    "v_exp_f32 v146, v146\n"
    "v_exp_f32 v147, v147\n"
    "s_nop 0\n"
    "v_cvt_pk_bf16_f32 v169, v146, v147\n"
    "s_mov_b32 s79, 7\n"
    "ATT_LOOP:\n"
    "v_xor_b32 v21, 0x10000, v21\n"
    "v_xor_b32 v22, 0x10000, v22\n"
    "v_xor_b32 v23, 0x10000, v23\n"
    "v_xor_b32 v24, 0x10000, v24\n"
    "s_waitcnt lgkmcnt(0)\n"
    "v_mfma_f32_16x16x32_bf16 v[112:115], a[192:195], v[32:35], v[172:175]\n"
    "v_mfma_f32_16x16x32_bf16 v[116:119], a[196:199], v[32:35], v[172:175]\n"
    "v_exp_f32 v148, v148\n"
    "v_mfma_f32_16x16x32_bf16 v[112:115], a[200:203], v[36:39], v[112:115]\n"
    "v_exp_f32 v149, v149\n"
    "v_mfma_f32_16x16x32_bf16 v[116:119], a[204:207], v[36:39], v[116:119]\n"
    "v_cvt_pk_bf16_f32 v170, v148, v149\n"
    "ds_read_b64_tr_b16 v[192:193], v25 offset:32768\n"
    "v_mfma_f32_16x16x32_bf16 v[112:115], a[208:211], v[40:43], v[112:115]\n"
    "ds_read_b64_tr_b16 v[194:195], v25 offset:36864\n"
    "s_add_u32 m0, s76, s78\n"
    "v_mfma_f32_16x16x32_bf16 v[116:119], a[212:215], v[40:43], v[116:119]\n"
    "v_exp_f32 v150, v150\n"
    "v_mfma_f32_16x16x32_bf16 v[112:115], a[216:219], v[44:47], v[112:115]\n"
    "v_exp_f32 v151, v151\n"
    "v_mfma_f32_16x16x32_bf16 v[116:119], a[220:223], v[44:47], v[116:119]\n"
    "v_cvt_pk_bf16_f32 v171, v150, v151\n"
    "ds_read_b64_tr_b16 v[196:197], v26 offset:32768\n"
    "v_mfma_f32_16x16x32_bf16 a[96:99], a[224:227], v[164:167], a[96:99]\n"
    "ds_read_b64_tr_b16 v[198:199], v26 offset:36864\n"
    "v_mfma_f32_16x16x32_bf16 a[100:103], a[228:231], v[164:167], a[100:103]\n"
    "buffer_load_dwordx4 v14, s[28:31], s75 offen offset:0 nt lds\n"
    "v_mfma_f32_16x16x32_bf16 a[104:107], a[232:235], v[164:167], a[104:107]\n"
    "v_exp_f32 v112, v112\n"
    "v_mfma_f32_16x16x32_bf16 a[108:111], a[236:239], v[164:167], a[108:111]\n"
    "v_exp_f32 v113, v113\n"
    "v_mfma_f32_16x16x32_bf16 a[112:115], a[240:243], v[164:167], a[112:115]\n"
    "v_cvt_pk_bf16_f32 v152, v112, v113\n"
    "ds_read_b64_tr_b16 v[200:201], v25 offset:33280\n"
    "v_mfma_f32_16x16x32_bf16 a[116:119], a[244:247], v[164:167], a[116:119]\n"
    "ds_read_b64_tr_b16 v[202:203], v25 offset:37376\n"
    "v_mfma_f32_16x16x32_bf16 a[120:123], a[248:251], v[164:167], a[120:123]\n"
    "v_exp_f32 v114, v114\n"
    "v_mfma_f32_16x16x32_bf16 a[124:127], a[252:255], v[164:167], a[124:127]\n"
    "v_exp_f32 v115, v115\n"
    "v_mfma_f32_16x16x32_bf16 a[172:175], a[180:183], v[164:167], a[172:175]\n"
    "v_cvt_pk_bf16_f32 v153, v114, v115\n"
    "ds_read_b64_tr_b16 v[204:205], v26 offset:33280\n"
    "v_mfma_f32_16x16x32_bf16 v[120:123], a[192:195], v[48:51], v[176:179]\n"
    "ds_read_b64_tr_b16 v[206:207], v26 offset:37376\n"
    "buffer_load_dwordx4 v15, s[28:31], s75 offen offset:1024 nt lds\n"
    "v_mfma_f32_16x16x32_bf16 v[124:127], a[196:199], v[48:51], v[176:179]\n"
    "v_exp_f32 v116, v116\n"
    "v_mfma_f32_16x16x32_bf16 v[120:123], a[200:203], v[52:55], v[120:123]\n"
    "v_exp_f32 v117, v117\n"
    "v_mfma_f32_16x16x32_bf16 v[124:127], a[204:207], v[52:55], v[124:127]\n"
    "v_cvt_pk_bf16_f32 v154, v116, v117\n"
    "ds_read_b64_tr_b16 v[208:209], v25 offset:33792\n"
    "v_mfma_f32_16x16x32_bf16 v[120:123], a[208:211], v[56:59], v[120:123]\n"
    "ds_read_b64_tr_b16 v[210:211], v25 offset:37888\n"
    "buffer_load_dwordx4 v16, s[28:31], s75 offen offset:2048 nt lds\n"
    "v_mfma_f32_16x16x32_bf16 v[124:127], a[212:215], v[56:59], v[124:127]\n"
    "v_exp_f32 v118, v118\n"
    "v_mfma_f32_16x16x32_bf16 v[120:123], a[216:219], v[60:63], v[120:123]\n"
    "v_exp_f32 v119, v119\n"
    "v_mfma_f32_16x16x32_bf16 v[124:127], a[220:223], v[60:63], v[124:127]\n"
    "v_cvt_pk_bf16_f32 v155, v118, v119\n"
    "ds_read_b64_tr_b16 v[212:213], v26 offset:33792\n"
    "v_mfma_f32_16x16x32_bf16 a[128:131], a[224:227], v[168:171], a[128:131]\n"
    "ds_read_b64_tr_b16 v[214:215], v26 offset:37888\n"
    "v_mfma_f32_16x16x32_bf16 a[132:135], a[228:231], v[168:171], a[132:135]\n"
    "buffer_load_dwordx4 v17, s[28:31], s75 offen offset:3072 nt lds\n"
    "v_mfma_f32_16x16x32_bf16 a[136:139], a[232:235], v[168:171], a[136:139]\n"
    "v_exp_f32 v120, v120\n"
    "v_mfma_f32_16x16x32_bf16 a[140:143], a[236:239], v[168:171], a[140:143]\n"
    "v_exp_f32 v121, v121\n"
    "v_mfma_f32_16x16x32_bf16 a[144:147], a[240:243], v[168:171], a[144:147]\n"
    "v_cvt_pk_bf16_f32 v156, v120, v121\n"
    "ds_read_b64_tr_b16 v[216:217], v25 offset:34304\n"
    "v_mfma_f32_16x16x32_bf16 a[148:151], a[244:247], v[168:171], a[148:151]\n"
    "ds_read_b64_tr_b16 v[218:219], v25 offset:38400\n"
    "v_mfma_f32_16x16x32_bf16 a[152:155], a[248:251], v[168:171], a[152:155]\n"
    "v_exp_f32 v122, v122\n"
    "v_mfma_f32_16x16x32_bf16 a[156:159], a[252:255], v[168:171], a[156:159]\n"
    "v_exp_f32 v123, v123\n"
    "v_mfma_f32_16x16x32_bf16 a[176:179], a[180:183], v[168:171], a[176:179]\n"
    "v_cvt_pk_bf16_f32 v157, v122, v123\n"
    "ds_read_b64_tr_b16 v[220:221], v26 offset:34304\n"
    "v_mfma_f32_16x16x32_bf16 v[128:131], a[192:195], v[64:67], v[180:183]\n"
    "ds_read_b64_tr_b16 v[222:223], v26 offset:38400\n"
    "v_mfma_f32_16x16x32_bf16 v[132:135], a[196:199], v[64:67], v[180:183]\n"
    "v_exp_f32 v124, v124\n"
    "v_mfma_f32_16x16x32_bf16 v[128:131], a[200:203], v[68:71], v[128:131]\n"
    "v_exp_f32 v125, v125\n"
    "v_mfma_f32_16x16x32_bf16 v[132:135], a[204:207], v[68:71], v[132:135]\n"
    "v_cvt_pk_bf16_f32 v158, v124, v125\n"
    "s_add_u32 s64, s76, s78\n"
    "v_mfma_f32_16x16x32_bf16 v[128:131], a[208:211], v[72:75], v[128:131]\n"
    "v_mfma_f32_16x16x32_bf16 v[132:135], a[212:215], v[72:75], v[132:135]\n"
    "v_exp_f32 v126, v126\n"
    "v_mfma_f32_16x16x32_bf16 v[128:131], a[216:219], v[76:79], v[128:131]\n"
    "v_exp_f32 v127, v127\n"
    "v_mfma_f32_16x16x32_bf16 v[132:135], a[220:223], v[76:79], v[132:135]\n"
    "v_cvt_pk_bf16_f32 v159, v126, v127\n"
    "s_add_u32 m0, s64, 16384\n"
    "s_waitcnt lgkmcnt(0)\n"
    "v_mfma_f32_16x16x32_bf16 a[0:3], v[192:195], v[152:155], a[0:3]\n"
    "buffer_load_dwordx4 v18, s[36:39], s75 offen offset:0 nt lds\n"
    "v_mfma_f32_16x16x32_bf16 a[4:7], v[196:199], v[152:155], a[4:7]\n"
    "v_mfma_f32_16x16x32_bf16 a[8:11], v[200:203], v[152:155], a[8:11]\n"
    "v_exp_f32 v128, v128\n"
    "v_mfma_f32_16x16x32_bf16 a[12:15], v[204:207], v[152:155], a[12:15]\n"
    "v_exp_f32 v129, v129\n"
    "v_mfma_f32_16x16x32_bf16 a[16:19], v[208:211], v[152:155], a[16:19]\n"
    "v_cvt_pk_bf16_f32 v160, v128, v129\n"
    "v_mfma_f32_16x16x32_bf16 a[20:23], v[212:215], v[152:155], a[20:23]\n"
    "buffer_load_dwordx4 v19, s[36:39], s75 offen offset:1024 nt lds\n"
    "v_mfma_f32_16x16x32_bf16 a[24:27], v[216:219], v[152:155], a[24:27]\n"
    "v_exp_f32 v130, v130\n"
    "v_mfma_f32_16x16x32_bf16 a[28:31], v[220:223], v[152:155], a[28:31]\n"
    "v_exp_f32 v131, v131\n"
    "v_mfma_f32_16x16x32_bf16 a[160:163], a[180:183], v[152:155], a[160:163]\n"
    "v_cvt_pk_bf16_f32 v161, v130, v131\n"
    "v_mfma_f32_16x16x32_bf16 v[136:139], a[192:195], v[80:83], v[184:187]\n"
    "buffer_load_dwordx4 v18, s[36:39], s75 offen offset:2048 nt lds\n"
    "v_mfma_f32_16x16x32_bf16 v[140:143], a[196:199], v[80:83], v[184:187]\n"
    "v_exp_f32 v132, v132\n"
    "v_mfma_f32_16x16x32_bf16 v[136:139], a[200:203], v[84:87], v[136:139]\n"
    "v_exp_f32 v133, v133\n"
    "v_mfma_f32_16x16x32_bf16 v[140:143], a[204:207], v[84:87], v[140:143]\n"
    "v_cvt_pk_bf16_f32 v162, v132, v133\n"
    "v_mfma_f32_16x16x32_bf16 v[136:139], a[208:211], v[88:91], v[136:139]\n"
    "buffer_load_dwordx4 v19, s[36:39], s75 offen offset:3072 nt lds\n"
    "v_mfma_f32_16x16x32_bf16 v[140:143], a[212:215], v[88:91], v[140:143]\n"
    "v_exp_f32 v134, v134\n"
    "v_mfma_f32_16x16x32_bf16 v[136:139], a[216:219], v[92:95], v[136:139]\n"
    "v_exp_f32 v135, v135\n"
    "v_mfma_f32_16x16x32_bf16 v[140:143], a[220:223], v[92:95], v[140:143]\n"
    "v_cvt_pk_bf16_f32 v163, v134, v135\n"
    "s_add_u32 s75, s75, 8192\n"
    "v_mfma_f32_16x16x32_bf16 a[32:35], v[192:195], v[156:159], a[32:35]\n"
    "v_mfma_f32_16x16x32_bf16 a[36:39], v[196:199], v[156:159], a[36:39]\n"
    "s_add_u32 s76, s76, 32768\n"
    "v_mfma_f32_16x16x32_bf16 a[40:43], v[200:203], v[156:159], a[40:43]\n"
    "v_exp_f32 v136, v136\n"
    "v_mfma_f32_16x16x32_bf16 a[44:47], v[204:207], v[156:159], a[44:47]\n"
    "v_exp_f32 v137, v137\n"
    "v_mfma_f32_16x16x32_bf16 a[48:51], v[208:211], v[156:159], a[48:51]\n"
    "v_cvt_pk_bf16_f32 v164, v136, v137\n"
    "s_and_b32 s76, s76, 0x1ffff\n"
    "v_mfma_f32_16x16x32_bf16 a[52:55], v[212:215], v[156:159], a[52:55]\n"
    "v_mfma_f32_16x16x32_bf16 a[56:59], v[216:219], v[156:159], a[56:59]\n"
    "v_exp_f32 v138, v138\n"
    "v_mfma_f32_16x16x32_bf16 a[60:63], v[220:223], v[156:159], a[60:63]\n"
    "v_exp_f32 v139, v139\n"
    "v_mfma_f32_16x16x32_bf16 a[164:167], a[180:183], v[156:159], a[164:167]\n"
    "v_cvt_pk_bf16_f32 v165, v138, v139\n"
    "s_waitcnt lgkmcnt(0)\n"
    "s_waitcnt vmcnt(16)\n"
    "s_barrier\n"
    "s_or_b32 s69, s58, s81\n"
    "s_cmp_lg_u32 s69, 0\n"
    "s_cbranch_scc1 HSRL_SKIP\n"
    "s_mov_b64 exec, 0xff\n"
    "s_waitcnt vmcnt(16)\n"
    "v_cmp_eq_u32 vcc, 1, v238\n"
    "s_nop 2\n"
    "v_cndmask_b32 v239, v239, v236, vcc\n"
    "v_cndmask_b32 v251, v238, 2, vcc\n"
    "v_cmp_eq_u32 s[88:89], s80, v237\n"
    "v_cmp_eq_u32 vcc, 0, v251\n"
    "s_nop 2\n"
    "s_and_b64 vcc, vcc, s[88:89]\n"
    "v_cndmask_b32 v238, v251, 1, vcc\n"
    "global_store_dword v242, v236, s[56:57] sc1\n"
    "v_cmp_eq_u32 vcc, 2, v238\n"
    "s_nop 2\n"
    "s_cmp_eq_u64 vcc, 0xff\n"
    "s_cselect_b32 s81, 1, 0\n"
    "global_load_dword v236, v240, s[56:57] sc1\n"
    "global_load_dword v237, v241, s[56:57] sc1\n"
    "s_mov_b64 exec, -1\n"
    "HSRL_SKIP:\n"
    "v_mfma_f32_16x16x32_bf16 v[144:147], a[192:195], v[96:99], v[188:191]\n"
    "v_mfma_f32_16x16x32_bf16 v[148:151], a[196:199], v[96:99], v[188:191]\n"
    "v_exp_f32 v140, v140\n"
    "v_mfma_f32_16x16x32_bf16 v[144:147], a[200:203], v[100:103], v[144:147]\n"
    "v_exp_f32 v141, v141\n"
    "v_mfma_f32_16x16x32_bf16 v[148:151], a[204:207], v[100:103], v[148:151]\n"
    "v_cvt_pk_bf16_f32 v166, v140, v141\n"
    "ds_read_b128 a[192:195], v21 offset:0\n"
    "v_mfma_f32_16x16x32_bf16 v[144:147], a[208:211], v[104:107], v[144:147]\n"
    "ds_read_b128 a[196:199], v21 offset:4096\n"
    "ds_read_b128 a[200:203], v22 offset:0\n"
    "v_mfma_f32_16x16x32_bf16 v[148:151], a[212:215], v[104:107], v[148:151]\n"
    "v_exp_f32 v142, v142\n"
    "v_mfma_f32_16x16x32_bf16 v[144:147], a[216:219], v[108:111], v[144:147]\n"
    "v_exp_f32 v143, v143\n"
    "v_mfma_f32_16x16x32_bf16 v[148:151], a[220:223], v[108:111], v[148:151]\n"
    "v_cvt_pk_bf16_f32 v167, v142, v143\n"
    "ds_read_b128 a[204:207], v22 offset:4096\n"
    "v_mfma_f32_16x16x32_bf16 a[64:67], v[192:195], v[160:163], a[64:67]\n"
    "ds_read_b128 a[208:211], v23 offset:0\n"
    "ds_read_b128 a[212:215], v23 offset:4096\n"
    "v_mfma_f32_16x16x32_bf16 a[68:71], v[196:199], v[160:163], a[68:71]\n"
    "ds_read_b128 a[216:219], v24 offset:0\n"
    "ds_read_b128 a[220:223], v24 offset:4096\n"
    "v_mfma_f32_16x16x32_bf16 a[72:75], v[200:203], v[160:163], a[72:75]\n"
    "v_exp_f32 v144, v144\n"
    "v_mfma_f32_16x16x32_bf16 a[76:79], v[204:207], v[160:163], a[76:79]\n"
    "v_exp_f32 v145, v145\n"
    "v_mfma_f32_16x16x32_bf16 a[80:83], v[208:211], v[160:163], a[80:83]\n"
    "v_cvt_pk_bf16_f32 v168, v144, v145\n"
    "v_mfma_f32_16x16x32_bf16 a[84:87], v[212:215], v[160:163], a[84:87]\n"
    "v_mfma_f32_16x16x32_bf16 a[88:91], v[216:219], v[160:163], a[88:91]\n"
    "v_exp_f32 v146, v146\n"
    "v_mfma_f32_16x16x32_bf16 a[92:95], v[220:223], v[160:163], a[92:95]\n"
    "v_exp_f32 v147, v147\n"
    "v_mfma_f32_16x16x32_bf16 a[168:171], a[180:183], v[160:163], a[168:171]\n"
    "v_cvt_pk_bf16_f32 v169, v146, v147\n"
    "v_xor_b32 v25, 0x10000, v25\n"
    "v_xor_b32 v26, 0x10000, v26\n"
    "s_waitcnt lgkmcnt(0)\n"
    "v_mfma_f32_16x16x32_bf16 v[112:115], a[192:195], v[32:35], v[172:175]\n"
    "v_mfma_f32_16x16x32_bf16 v[116:119], a[196:199], v[32:35], v[172:175]\n"
    "v_exp_f32 v148, v148\n"
    "v_mfma_f32_16x16x32_bf16 v[112:115], a[200:203], v[36:39], v[112:115]\n"
    "v_exp_f32 v149, v149\n"
    "v_mfma_f32_16x16x32_bf16 v[116:119], a[204:207], v[36:39], v[116:119]\n"
    "v_cvt_pk_bf16_f32 v170, v148, v149\n"
    "ds_read_b64_tr_b16 a[224:225], v25 offset:0\n"
    "v_mfma_f32_16x16x32_bf16 v[112:115], a[208:211], v[40:43], v[112:115]\n"
    "ds_read_b64_tr_b16 a[226:227], v25 offset:4096\n"
    "s_add_u32 m0, s76, s78\n"
    "v_mfma_f32_16x16x32_bf16 v[116:119], a[212:215], v[40:43], v[116:119]\n"
    "v_exp_f32 v150, v150\n"
    "v_mfma_f32_16x16x32_bf16 v[112:115], a[216:219], v[44:47], v[112:115]\n"
    "v_exp_f32 v151, v151\n"
    "v_mfma_f32_16x16x32_bf16 v[116:119], a[220:223], v[44:47], v[116:119]\n"
    "v_cvt_pk_bf16_f32 v171, v150, v151\n"
    "ds_read_b64_tr_b16 a[228:229], v26 offset:0\n"
    "v_mfma_f32_16x16x32_bf16 a[96:99], v[192:195], v[164:167], a[96:99]\n"
    "ds_read_b64_tr_b16 a[230:231], v26 offset:4096\n"
    "v_mfma_f32_16x16x32_bf16 a[100:103], v[196:199], v[164:167], a[100:103]\n"
    "buffer_load_dwordx4 v14, s[28:31], s75 offen offset:0 nt lds\n"
    "v_mfma_f32_16x16x32_bf16 a[104:107], v[200:203], v[164:167], a[104:107]\n"
    "v_exp_f32 v112, v112\n"
    "v_mfma_f32_16x16x32_bf16 a[108:111], v[204:207], v[164:167], a[108:111]\n"
    "v_exp_f32 v113, v113\n"
    "v_mfma_f32_16x16x32_bf16 a[112:115], v[208:211], v[164:167], a[112:115]\n"
    "v_cvt_pk_bf16_f32 v152, v112, v113\n"
    "ds_read_b64_tr_b16 a[232:233], v25 offset:512\n"
    "v_mfma_f32_16x16x32_bf16 a[116:119], v[212:215], v[164:167], a[116:119]\n"
    "ds_read_b64_tr_b16 a[234:235], v25 offset:4608\n"
    "v_mfma_f32_16x16x32_bf16 a[120:123], v[216:219], v[164:167], a[120:123]\n"
    "v_exp_f32 v114, v114\n"
    "v_mfma_f32_16x16x32_bf16 a[124:127], v[220:223], v[164:167], a[124:127]\n"
    "v_exp_f32 v115, v115\n"
    "v_mfma_f32_16x16x32_bf16 a[172:175], a[180:183], v[164:167], a[172:175]\n"
    "v_cvt_pk_bf16_f32 v153, v114, v115\n"
    "ds_read_b64_tr_b16 a[236:237], v26 offset:512\n"
    "v_mfma_f32_16x16x32_bf16 v[120:123], a[192:195], v[48:51], v[176:179]\n"
    "ds_read_b64_tr_b16 a[238:239], v26 offset:4608\n"
    "buffer_load_dwordx4 v15, s[28:31], s75 offen offset:1024 nt lds\n"
    "v_mfma_f32_16x16x32_bf16 v[124:127], a[196:199], v[48:51], v[176:179]\n"
    "v_exp_f32 v116, v116\n"
    "v_mfma_f32_16x16x32_bf16 v[120:123], a[200:203], v[52:55], v[120:123]\n"
    "v_exp_f32 v117, v117\n"
    "v_mfma_f32_16x16x32_bf16 v[124:127], a[204:207], v[52:55], v[124:127]\n"
    "v_cvt_pk_bf16_f32 v154, v116, v117\n"
    "ds_read_b64_tr_b16 a[240:241], v25 offset:1024\n"
    "v_mfma_f32_16x16x32_bf16 v[120:123], a[208:211], v[56:59], v[120:123]\n"
    "ds_read_b64_tr_b16 a[242:243], v25 offset:5120\n"
    "buffer_load_dwordx4 v16, s[28:31], s75 offen offset:2048 nt lds\n"
    "v_mfma_f32_16x16x32_bf16 v[124:127], a[212:215], v[56:59], v[124:127]\n"
    "v_exp_f32 v118, v118\n"
    "v_mfma_f32_16x16x32_bf16 v[120:123], a[216:219], v[60:63], v[120:123]\n"
    "v_exp_f32 v119, v119\n"
    "v_mfma_f32_16x16x32_bf16 v[124:127], a[220:223], v[60:63], v[124:127]\n"
    "v_cvt_pk_bf16_f32 v155, v118, v119\n"
    "ds_read_b64_tr_b16 a[244:245], v26 offset:1024\n"
    "v_mfma_f32_16x16x32_bf16 a[128:131], v[192:195], v[168:171], a[128:131]\n"
    "ds_read_b64_tr_b16 a[246:247], v26 offset:5120\n"
    "v_mfma_f32_16x16x32_bf16 a[132:135], v[196:199], v[168:171], a[132:135]\n"
    "buffer_load_dwordx4 v17, s[28:31], s75 offen offset:3072 nt lds\n"
    "v_mfma_f32_16x16x32_bf16 a[136:139], v[200:203], v[168:171], a[136:139]\n"
    "v_exp_f32 v120, v120\n"
    "v_mfma_f32_16x16x32_bf16 a[140:143], v[204:207], v[168:171], a[140:143]\n"
    "v_exp_f32 v121, v121\n"
    "v_mfma_f32_16x16x32_bf16 a[144:147], v[208:211], v[168:171], a[144:147]\n"
    "v_cvt_pk_bf16_f32 v156, v120, v121\n"
    "ds_read_b64_tr_b16 a[248:249], v25 offset:1536\n"
    "v_mfma_f32_16x16x32_bf16 a[148:151], v[212:215], v[168:171], a[148:151]\n"
    "ds_read_b64_tr_b16 a[250:251], v25 offset:5632\n"
    "v_mfma_f32_16x16x32_bf16 a[152:155], v[216:219], v[168:171], a[152:155]\n"
    "v_exp_f32 v122, v122\n"
    "v_mfma_f32_16x16x32_bf16 a[156:159], v[220:223], v[168:171], a[156:159]\n"
    "v_exp_f32 v123, v123\n"
    "v_mfma_f32_16x16x32_bf16 a[176:179], a[180:183], v[168:171], a[176:179]\n"
    "v_cvt_pk_bf16_f32 v157, v122, v123\n"
    "ds_read_b64_tr_b16 a[252:253], v26 offset:1536\n"
    "v_mfma_f32_16x16x32_bf16 v[128:131], a[192:195], v[64:67], v[180:183]\n"
    "ds_read_b64_tr_b16 a[254:255], v26 offset:5632\n"
    "v_mfma_f32_16x16x32_bf16 v[132:135], a[196:199], v[64:67], v[180:183]\n"
    "v_exp_f32 v124, v124\n"
    "v_mfma_f32_16x16x32_bf16 v[128:131], a[200:203], v[68:71], v[128:131]\n"
    "v_exp_f32 v125, v125\n"
    "v_mfma_f32_16x16x32_bf16 v[132:135], a[204:207], v[68:71], v[132:135]\n"
    "v_cvt_pk_bf16_f32 v158, v124, v125\n"
    "s_add_u32 s64, s76, s78\n"
    "v_mfma_f32_16x16x32_bf16 v[128:131], a[208:211], v[72:75], v[128:131]\n"
    "v_mfma_f32_16x16x32_bf16 v[132:135], a[212:215], v[72:75], v[132:135]\n"
    "v_exp_f32 v126, v126\n"
    "v_mfma_f32_16x16x32_bf16 v[128:131], a[216:219], v[76:79], v[128:131]\n"
    "v_exp_f32 v127, v127\n"
    "v_mfma_f32_16x16x32_bf16 v[132:135], a[220:223], v[76:79], v[132:135]\n"
    "v_cvt_pk_bf16_f32 v159, v126, v127\n"
    "s_add_u32 m0, s64, 16384\n"
    "s_waitcnt lgkmcnt(0)\n"
    "v_mfma_f32_16x16x32_bf16 a[0:3], a[224:227], v[152:155], a[0:3]\n"
    "buffer_load_dwordx4 v18, s[36:39], s75 offen offset:0 nt lds\n"
    "v_mfma_f32_16x16x32_bf16 a[4:7], a[228:231], v[152:155], a[4:7]\n"
    "v_mfma_f32_16x16x32_bf16 a[8:11], a[232:235], v[152:155], a[8:11]\n"
    "v_exp_f32 v128, v128\n"
    "v_mfma_f32_16x16x32_bf16 a[12:15], a[236:239], v[152:155], a[12:15]\n"
    "v_exp_f32 v129, v129\n"
    "v_mfma_f32_16x16x32_bf16 a[16:19], a[240:243], v[152:155], a[16:19]\n"
    "v_cvt_pk_bf16_f32 v160, v128, v129\n"
    "v_mfma_f32_16x16x32_bf16 a[20:23], a[244:247], v[152:155], a[20:23]\n"
    "buffer_load_dwordx4 v19, s[36:39], s75 offen offset:1024 nt lds\n"
    "v_mfma_f32_16x16x32_bf16 a[24:27], a[248:251], v[152:155], a[24:27]\n"
    "v_exp_f32 v130, v130\n"
    "v_mfma_f32_16x16x32_bf16 a[28:31], a[252:255], v[152:155], a[28:31]\n"
    "v_exp_f32 v131, v131\n"
    "v_mfma_f32_16x16x32_bf16 a[160:163], a[180:183], v[152:155], a[160:163]\n"
    "v_cvt_pk_bf16_f32 v161, v130, v131\n"
    "v_mfma_f32_16x16x32_bf16 v[136:139], a[192:195], v[80:83], v[184:187]\n"
    "buffer_load_dwordx4 v18, s[36:39], s75 offen offset:2048 nt lds\n"
    "v_mfma_f32_16x16x32_bf16 v[140:143], a[196:199], v[80:83], v[184:187]\n"
    "v_exp_f32 v132, v132\n"
    "v_mfma_f32_16x16x32_bf16 v[136:139], a[200:203], v[84:87], v[136:139]\n"
    "v_exp_f32 v133, v133\n"
    "v_mfma_f32_16x16x32_bf16 v[140:143], a[204:207], v[84:87], v[140:143]\n"
    "v_cvt_pk_bf16_f32 v162, v132, v133\n"
    "v_mfma_f32_16x16x32_bf16 v[136:139], a[208:211], v[88:91], v[136:139]\n"
    "buffer_load_dwordx4 v19, s[36:39], s75 offen offset:3072 nt lds\n"
    "v_mfma_f32_16x16x32_bf16 v[140:143], a[212:215], v[88:91], v[140:143]\n"
    "v_exp_f32 v134, v134\n"
    "v_mfma_f32_16x16x32_bf16 v[136:139], a[216:219], v[92:95], v[136:139]\n"
    "v_exp_f32 v135, v135\n"
    "v_mfma_f32_16x16x32_bf16 v[140:143], a[220:223], v[92:95], v[140:143]\n"
    "v_cvt_pk_bf16_f32 v163, v134, v135\n"
    "s_add_u32 s75, s75, 8192\n"
    "v_mfma_f32_16x16x32_bf16 a[32:35], a[224:227], v[156:159], a[32:35]\n"
    "v_mfma_f32_16x16x32_bf16 a[36:39], a[228:231], v[156:159], a[36:39]\n"
    "s_add_u32 s76, s76, 32768\n"
    "v_mfma_f32_16x16x32_bf16 a[40:43], a[232:235], v[156:159], a[40:43]\n"
    "v_exp_f32 v136, v136\n"
    "v_mfma_f32_16x16x32_bf16 a[44:47], a[236:239], v[156:159], a[44:47]\n"
    "v_exp_f32 v137, v137\n"
    "v_mfma_f32_16x16x32_bf16 a[48:51], a[240:243], v[156:159], a[48:51]\n"
    "v_cvt_pk_bf16_f32 v164, v136, v137\n"
    "s_and_b32 s76, s76, 0x1ffff\n"
    "v_mfma_f32_16x16x32_bf16 a[52:55], a[244:247], v[156:159], a[52:55]\n"
    "v_mfma_f32_16x16x32_bf16 a[56:59], a[248:251], v[156:159], a[56:59]\n"
    "v_exp_f32 v138, v138\n"
    "v_mfma_f32_16x16x32_bf16 a[60:63], a[252:255], v[156:159], a[60:63]\n"
    "v_exp_f32 v139, v139\n"
    "v_mfma_f32_16x16x32_bf16 a[164:167], a[180:183], v[156:159], a[164:167]\n"
    "v_cvt_pk_bf16_f32 v165, v138, v139\n"
    "s_waitcnt lgkmcnt(0)\n"
    "s_waitcnt vmcnt(16)\n"
    "s_barrier\n"
    "v_mfma_f32_16x16x32_bf16 v[144:147], a[192:195], v[96:99], v[188:191]\n"
    "v_mfma_f32_16x16x32_bf16 v[148:151], a[196:199], v[96:99], v[188:191]\n"
    "v_exp_f32 v140, v140\n"
    "v_mfma_f32_16x16x32_bf16 v[144:147], a[200:203], v[100:103], v[144:147]\n"
    "v_exp_f32 v141, v141\n"
    "v_mfma_f32_16x16x32_bf16 v[148:151], a[204:207], v[100:103], v[148:151]\n"
    "v_cvt_pk_bf16_f32 v166, v140, v141\n"
    "ds_read_b128 a[192:195], v21 offset:32768\n"
    "v_mfma_f32_16x16x32_bf16 v[144:147], a[208:211], v[104:107], v[144:147]\n"
    "ds_read_b128 a[196:199], v21 offset:36864\n"
    "ds_read_b128 a[200:203], v22 offset:32768\n"
    "v_mfma_f32_16x16x32_bf16 v[148:151], a[212:215], v[104:107], v[148:151]\n"
    "v_exp_f32 v142, v142\n"
    "v_mfma_f32_16x16x32_bf16 v[144:147], a[216:219], v[108:111], v[144:147]\n"
    "v_exp_f32 v143, v143\n"
    "v_mfma_f32_16x16x32_bf16 v[148:151], a[220:223], v[108:111], v[148:151]\n"
    "v_cvt_pk_bf16_f32 v167, v142, v143\n"
    "ds_read_b128 a[204:207], v22 offset:36864\n"
    "v_mfma_f32_16x16x32_bf16 a[64:67], a[224:227], v[160:163], a[64:67]\n"
    "ds_read_b128 a[208:211], v23 offset:32768\n"
    "ds_read_b128 a[212:215], v23 offset:36864\n"
    "v_mfma_f32_16x16x32_bf16 a[68:71], a[228:231], v[160:163], a[68:71]\n"
    "ds_read_b128 a[216:219], v24 offset:32768\n"
    "ds_read_b128 a[220:223], v24 offset:36864\n"
    "v_mfma_f32_16x16x32_bf16 a[72:75], a[232:235], v[160:163], a[72:75]\n"
    "v_exp_f32 v144, v144\n"
    "v_mfma_f32_16x16x32_bf16 a[76:79], a[236:239], v[160:163], a[76:79]\n"
    "v_exp_f32 v145, v145\n"
    "v_mfma_f32_16x16x32_bf16 a[80:83], a[240:243], v[160:163], a[80:83]\n"
    "v_cvt_pk_bf16_f32 v168, v144, v145\n"
    "v_mfma_f32_16x16x32_bf16 a[84:87], a[244:247], v[160:163], a[84:87]\n"
    "v_mfma_f32_16x16x32_bf16 a[88:91], a[248:251], v[160:163], a[88:91]\n"
    "v_exp_f32 v146, v146\n"
    "v_mfma_f32_16x16x32_bf16 a[92:95], a[252:255], v[160:163], a[92:95]\n"
    "v_exp_f32 v147, v147\n"
    "v_mfma_f32_16x16x32_bf16 a[168:171], a[180:183], v[160:163], a[168:171]\n"
    "v_cvt_pk_bf16_f32 v169, v146, v147\n"
    "s_sub_u32 s79, s79, 1\n"
    "s_cmp_lg_u32 s79, 0\n"
    "s_cbranch_scc1 ATT_LOOP\n"
    "s_waitcnt lgkmcnt(0)\n"
    "s_waitcnt vmcnt(0)\n"
    "v_mfma_f32_16x16x32_bf16 v[112:115], a[192:195], v[32:35], v[172:175]\n"
    "v_mfma_f32_16x16x32_bf16 v[116:119], a[196:199], v[32:35], v[172:175]\n"
    "v_exp_f32 v148, v148\n"
    "v_mfma_f32_16x16x32_bf16 v[112:115], a[200:203], v[36:39], v[112:115]\n"
    "v_exp_f32 v149, v149\n"
    "v_mfma_f32_16x16x32_bf16 v[116:119], a[204:207], v[36:39], v[116:119]\n"
    "v_cvt_pk_bf16_f32 v170, v148, v149\n"
    "ds_read_b64_tr_b16 v[192:193], v25 offset:32768\n"
    "v_mfma_f32_16x16x32_bf16 v[112:115], a[208:211], v[40:43], v[112:115]\n"
    "ds_read_b64_tr_b16 v[194:195], v25 offset:36864\n"
    "v_lshrrev_b32 v250, 2, v1\n"
    "v_mfma_f32_16x16x32_bf16 v[116:119], a[212:215], v[40:43], v[116:119]\n"
    "v_exp_f32 v150, v150\n"
    "v_mfma_f32_16x16x32_bf16 v[112:115], a[216:219], v[44:47], v[112:115]\n"
    "v_exp_f32 v151, v151\n"
    "v_mfma_f32_16x16x32_bf16 v[116:119], a[220:223], v[44:47], v[116:119]\n"
    "v_cvt_pk_bf16_f32 v171, v150, v151\n"
    "ds_read_b64_tr_b16 v[196:197], v26 offset:32768\n"
    "v_mfma_f32_16x16x32_bf16 a[96:99], a[224:227], v[164:167], a[96:99]\n"
    "ds_read_b64_tr_b16 v[198:199], v26 offset:36864\n"
    "v_and_b32 v251, 3, v1\n"
    "v_mfma_f32_16x16x32_bf16 a[100:103], a[228:231], v[164:167], a[100:103]\n"
    "v_lshlrev_b32 v251, 7, v251\n"
    "v_lshl_add_u32 v250, v250, 10, v251\n"
    "s_lshr_b32 s67, s61, 5\n"
    "v_mfma_f32_16x16x32_bf16 a[104:107], a[232:235], v[164:167], a[104:107]\n"
    "v_exp_f32 v112, v112\n"
    "s_lshl_b32 s67, s67, 2\n"
    "v_mfma_f32_16x16x32_bf16 a[108:111], a[236:239], v[164:167], a[108:111]\n"
    "v_exp_f32 v113, v113\n"
    "s_add_u32 s67, s67, s58\n"
    "v_mfma_f32_16x16x32_bf16 a[112:115], a[240:243], v[164:167], a[112:115]\n"
    "v_cvt_pk_bf16_f32 v152, v112, v113\n"
    "ds_read_b64_tr_b16 v[200:201], v25 offset:33280\n"
    "s_lshl_b32 s67, s67, 14\n"
    "s_add_u32 s67, s67, 4096\n"
    "buffer_load_dword v243, v250, s[40:43], s67 offen\n"
    "v_mfma_f32_16x16x32_bf16 a[116:119], a[244:247], v[164:167], a[116:119]\n"
    "ds_read_b64_tr_b16 v[202:203], v25 offset:37376\n"
    "v_mfma_f32_16x16x32_bf16 a[120:123], a[248:251], v[164:167], a[120:123]\n"
    "v_exp_f32 v114, v114\n"
    "v_mfma_f32_16x16x32_bf16 a[124:127], a[252:255], v[164:167], a[124:127]\n"
    "v_exp_f32 v115, v115\n"
    "v_mfma_f32_16x16x32_bf16 a[172:175], a[180:183], v[164:167], a[172:175]\n"
    "v_cvt_pk_bf16_f32 v153, v114, v115\n"
    "ds_read_b64_tr_b16 v[204:205], v26 offset:33280\n"
    "v_mfma_f32_16x16x32_bf16 v[120:123], a[192:195], v[48:51], v[176:179]\n"
    "ds_read_b64_tr_b16 v[206:207], v26 offset:37376\n"
    "s_add_u32 s64, s63, s59\n"
    "v_mfma_f32_16x16x32_bf16 v[124:127], a[196:199], v[48:51], v[176:179]\n"
    "v_exp_f32 v116, v116\n"
    "v_mfma_f32_16x16x32_bf16 v[120:123], a[200:203], v[52:55], v[120:123]\n"
    "v_exp_f32 v117, v117\n"
    "v_mfma_f32_16x16x32_bf16 v[124:127], a[204:207], v[52:55], v[124:127]\n"
    "v_cvt_pk_bf16_f32 v154, v116, v117\n"
    "ds_read_b64_tr_b16 v[208:209], v25 offset:33792\n"
    "v_mfma_f32_16x16x32_bf16 v[120:123], a[208:211], v[56:59], v[120:123]\n"
    "ds_read_b64_tr_b16 v[210:211], v25 offset:37888\n"
    "s_xor_b32 s64, s64, 0\n"
    "v_mfma_f32_16x16x32_bf16 v[124:127], a[212:215], v[56:59], v[124:127]\n"
    "v_exp_f32 v118, v118\n"
    "v_mfma_f32_16x16x32_bf16 v[120:123], a[216:219], v[60:63], v[120:123]\n"
    "v_exp_f32 v119, v119\n"
    "v_mfma_f32_16x16x32_bf16 v[124:127], a[220:223], v[60:63], v[124:127]\n"
    "v_cvt_pk_bf16_f32 v155, v118, v119\n"
    "ds_read_b64_tr_b16 v[212:213], v26 offset:33792\n"
    "v_mfma_f32_16x16x32_bf16 a[128:131], a[224:227], v[168:171], a[128:131]\n"
    "ds_read_b64_tr_b16 v[214:215], v26 offset:37888\n"
    "s_lshl_b32 s64, s64, 17\n"
    "v_mfma_f32_16x16x32_bf16 a[132:135], a[228:231], v[168:171], a[132:135]\n"
    "s_add_u32 s64, s64, s86\n"
    "s_add_u32 s64, s64, 65536\n"
    "v_mfma_f32_16x16x32_bf16 a[136:139], a[232:235], v[168:171], a[136:139]\n"
    "v_exp_f32 v120, v120\n"
    "v_mfma_f32_16x16x32_bf16 a[140:143], a[236:239], v[168:171], a[140:143]\n"
    "v_exp_f32 v121, v121\n"
    "v_mfma_f32_16x16x32_bf16 a[144:147], a[240:243], v[168:171], a[144:147]\n"
    "v_cvt_pk_bf16_f32 v156, v120, v121\n"
    "ds_read_b64_tr_b16 v[216:217], v25 offset:34304\n"
    "v_mfma_f32_16x16x32_bf16 a[148:151], a[244:247], v[168:171], a[148:151]\n"
    "ds_read_b64_tr_b16 v[218:219], v25 offset:38400\n"
    "s_add_u32 m0, s78, 0\n"
    "v_mfma_f32_16x16x32_bf16 a[152:155], a[248:251], v[168:171], a[152:155]\n"
    "v_exp_f32 v122, v122\n"
    "v_mfma_f32_16x16x32_bf16 a[156:159], a[252:255], v[168:171], a[156:159]\n"
    "v_exp_f32 v123, v123\n"
    "v_mfma_f32_16x16x32_bf16 a[176:179], a[180:183], v[168:171], a[176:179]\n"
    "v_cvt_pk_bf16_f32 v157, v122, v123\n"
    "ds_read_b64_tr_b16 v[220:221], v26 offset:34304\n"
    "v_mfma_f32_16x16x32_bf16 v[128:131], a[192:195], v[64:67], v[180:183]\n"
    "ds_read_b64_tr_b16 v[222:223], v26 offset:38400\n"
    "buffer_load_dwordx4 v7, s[40:43], s64 offen offset:0 lds\n"
    "v_mfma_f32_16x16x32_bf16 v[132:135], a[196:199], v[64:67], v[180:183]\n"
    "v_exp_f32 v124, v124\n"
    "v_mfma_f32_16x16x32_bf16 v[128:131], a[200:203], v[68:71], v[128:131]\n"
    "v_exp_f32 v125, v125\n"
    "v_mfma_f32_16x16x32_bf16 v[132:135], a[204:207], v[68:71], v[132:135]\n"
    "v_cvt_pk_bf16_f32 v158, v124, v125\n"
    "v_mfma_f32_16x16x32_bf16 v[128:131], a[208:211], v[72:75], v[128:131]\n"
    "buffer_load_dwordx4 v8, s[40:43], s64 offen offset:1024 lds\n"
    "v_mfma_f32_16x16x32_bf16 v[132:135], a[212:215], v[72:75], v[132:135]\n"
    "v_exp_f32 v126, v126\n"
    "v_mfma_f32_16x16x32_bf16 v[128:131], a[216:219], v[76:79], v[128:131]\n"
    "v_exp_f32 v127, v127\n"
    "v_mfma_f32_16x16x32_bf16 v[132:135], a[220:223], v[76:79], v[132:135]\n"
    "v_cvt_pk_bf16_f32 v159, v126, v127\n"
    "s_waitcnt lgkmcnt(0)\n"
    "v_mfma_f32_16x16x32_bf16 a[0:3], v[192:195], v[152:155], a[0:3]\n"
    "buffer_load_dwordx4 v9, s[40:43], s64 offen offset:2048 lds\n"
    "v_mfma_f32_16x16x32_bf16 a[4:7], v[196:199], v[152:155], a[4:7]\n"
    "buffer_load_dwordx4 v10, s[40:43], s64 offen offset:3072 lds\n"
    "v_mfma_f32_16x16x32_bf16 a[8:11], v[200:203], v[152:155], a[8:11]\n"
    "v_exp_f32 v128, v128\n"
    "v_mfma_f32_16x16x32_bf16 a[12:15], v[204:207], v[152:155], a[12:15]\n"
    "v_exp_f32 v129, v129\n"
    "v_mfma_f32_16x16x32_bf16 a[16:19], v[208:211], v[152:155], a[16:19]\n"
    "v_cvt_pk_bf16_f32 v160, v128, v129\n"
    "s_add_u32 s64, s63, s59\n"
    "v_mfma_f32_16x16x32_bf16 a[20:23], v[212:215], v[152:155], a[20:23]\n"
    "s_xor_b32 s64, s64, 0\n"
    "s_lshl_b32 s64, s64, 17\n"
    "v_mfma_f32_16x16x32_bf16 a[24:27], v[216:219], v[152:155], a[24:27]\n"
    "v_exp_f32 v130, v130\n"
    "v_mfma_f32_16x16x32_bf16 a[28:31], v[220:223], v[152:155], a[28:31]\n"
    "v_exp_f32 v131, v131\n"
    "v_mfma_f32_16x16x32_bf16 a[160:163], a[180:183], v[152:155], a[160:163]\n"
    "v_cvt_pk_bf16_f32 v161, v130, v131\n"
    "s_add_u32 s64, s64, s86\n"
    "v_mfma_f32_16x16x32_bf16 v[136:139], a[192:195], v[80:83], v[184:187]\n"
    "s_add_u32 s64, s64, 98304\n"
    "s_add_u32 m0, s78, 16384\n"
    "v_mfma_f32_16x16x32_bf16 v[140:143], a[196:199], v[80:83], v[184:187]\n"
    "v_exp_f32 v132, v132\n"
    "v_mfma_f32_16x16x32_bf16 v[136:139], a[200:203], v[84:87], v[136:139]\n"
    "v_exp_f32 v133, v133\n"
    "v_mfma_f32_16x16x32_bf16 v[140:143], a[204:207], v[84:87], v[140:143]\n"
    "v_cvt_pk_bf16_f32 v162, v132, v133\n"
    "v_mfma_f32_16x16x32_bf16 v[136:139], a[208:211], v[88:91], v[136:139]\n"
    "buffer_load_dwordx4 v7, s[40:43], s64 offen offset:0 lds\n"
    "v_mfma_f32_16x16x32_bf16 v[140:143], a[212:215], v[88:91], v[140:143]\n"
    "v_exp_f32 v134, v134\n"
    "v_mfma_f32_16x16x32_bf16 v[136:139], a[216:219], v[92:95], v[136:139]\n"
    "v_exp_f32 v135, v135\n"
    "v_mfma_f32_16x16x32_bf16 v[140:143], a[220:223], v[92:95], v[140:143]\n"
    "v_cvt_pk_bf16_f32 v163, v134, v135\n"
    "v_mfma_f32_16x16x32_bf16 a[32:35], v[192:195], v[156:159], a[32:35]\n"
    "buffer_load_dwordx4 v8, s[40:43], s64 offen offset:1024 lds\n"
    "v_mfma_f32_16x16x32_bf16 a[36:39], v[196:199], v[156:159], a[36:39]\n"
    "buffer_load_dwordx4 v9, s[40:43], s64 offen offset:2048 lds\n"
    "v_mfma_f32_16x16x32_bf16 a[40:43], v[200:203], v[156:159], a[40:43]\n"
    "v_exp_f32 v136, v136\n"
    "v_mfma_f32_16x16x32_bf16 a[44:47], v[204:207], v[156:159], a[44:47]\n"
    "v_exp_f32 v137, v137\n"
    "v_mfma_f32_16x16x32_bf16 a[48:51], v[208:211], v[156:159], a[48:51]\n"
    "v_cvt_pk_bf16_f32 v164, v136, v137\n"
    "v_mfma_f32_16x16x32_bf16 a[52:55], v[212:215], v[156:159], a[52:55]\n"
    "buffer_load_dwordx4 v10, s[40:43], s64 offen offset:3072 lds\n"
    "v_mfma_f32_16x16x32_bf16 a[56:59], v[216:219], v[156:159], a[56:59]\n"
    "v_exp_f32 v138, v138\n"
    "v_mfma_f32_16x16x32_bf16 a[60:63], v[220:223], v[156:159], a[60:63]\n"
    "v_exp_f32 v139, v139\n"
    "v_mfma_f32_16x16x32_bf16 a[164:167], a[180:183], v[156:159], a[164:167]\n"
    "v_cvt_pk_bf16_f32 v165, v138, v139\n"
    "s_add_u32 s64, s63, s59\n"
    "s_waitcnt lgkmcnt(0)\n"
    "s_waitcnt vmcnt(0)\n"
    "s_barrier\n"
    "v_mfma_f32_16x16x32_bf16 v[144:147], a[192:195], v[96:99], v[188:191]\n"
    "s_xor_b32 s64, s64, 1\n"
    "s_lshl_b32 s64, s64, 17\n"
    "v_mfma_f32_16x16x32_bf16 v[148:151], a[196:199], v[96:99], v[188:191]\n"
    "v_exp_f32 v140, v140\n"
    "v_mfma_f32_16x16x32_bf16 v[144:147], a[200:203], v[100:103], v[144:147]\n"
    "v_exp_f32 v141, v141\n"
    "v_mfma_f32_16x16x32_bf16 v[148:151], a[204:207], v[100:103], v[148:151]\n"
    "v_cvt_pk_bf16_f32 v166, v140, v141\n"
    "s_add_u32 s64, s64, s86\n"
    "v_mfma_f32_16x16x32_bf16 v[144:147], a[208:211], v[104:107], v[144:147]\n"
    "s_add_u32 s64, s64, 0\n"
    "s_add_u32 m0, s78, 32768\n"
    "v_mfma_f32_16x16x32_bf16 v[148:151], a[212:215], v[104:107], v[148:151]\n"
    "v_exp_f32 v142, v142\n"
    "v_mfma_f32_16x16x32_bf16 v[144:147], a[216:219], v[108:111], v[144:147]\n"
    "v_exp_f32 v143, v143\n"
    "v_mfma_f32_16x16x32_bf16 v[148:151], a[220:223], v[108:111], v[148:151]\n"
    "v_cvt_pk_bf16_f32 v167, v142, v143\n"
    "v_mfma_f32_16x16x32_bf16 a[64:67], v[192:195], v[160:163], a[64:67]\n"
    "buffer_load_dwordx4 v7, s[40:43], s64 offen offset:0 lds\n"
    "buffer_load_dwordx4 v8, s[40:43], s64 offen offset:1024 lds\n"
    "buffer_load_dwordx4 v9, s[40:43], s64 offen offset:2048 lds\n"
    "buffer_load_dwordx4 v10, s[40:43], s64 offen offset:3072 lds\n"
    "v_mfma_f32_16x16x32_bf16 a[68:71], v[196:199], v[160:163], a[68:71]\n"
    "v_mfma_f32_16x16x32_bf16 a[72:75], v[200:203], v[160:163], a[72:75]\n"
    "v_exp_f32 v144, v144\n"
    "v_mfma_f32_16x16x32_bf16 a[76:79], v[204:207], v[160:163], a[76:79]\n"
    "v_exp_f32 v145, v145\n"
    "v_mfma_f32_16x16x32_bf16 a[80:83], v[208:211], v[160:163], a[80:83]\n"
    "v_cvt_pk_bf16_f32 v168, v144, v145\n"
    "v_mfma_f32_16x16x32_bf16 a[84:87], v[212:215], v[160:163], a[84:87]\n"
    "v_mfma_f32_16x16x32_bf16 a[88:91], v[216:219], v[160:163], a[88:91]\n"
    "v_exp_f32 v146, v146\n"
    "v_mfma_f32_16x16x32_bf16 a[92:95], v[220:223], v[160:163], a[92:95]\n"
    "v_exp_f32 v147, v147\n"
    "v_mfma_f32_16x16x32_bf16 a[168:171], a[180:183], v[160:163], a[168:171]\n"
    "v_cvt_pk_bf16_f32 v169, v146, v147\n"
    "v_mfma_f32_16x16x32_bf16 a[96:99], v[192:195], v[164:167], a[96:99]\n"
    "v_exp_f32 v148, v148\n"
    "v_mfma_f32_16x16x32_bf16 a[100:103], v[196:199], v[164:167], a[100:103]\n"
    "v_exp_f32 v149, v149\n"
    "v_mfma_f32_16x16x32_bf16 a[104:107], v[200:203], v[164:167], a[104:107]\n"
    "v_cvt_pk_bf16_f32 v170, v148, v149\n"
    "v_mfma_f32_16x16x32_bf16 a[108:111], v[204:207], v[164:167], a[108:111]\n"
    "v_exp_f32 v150, v150\n"
    "v_mfma_f32_16x16x32_bf16 a[112:115], v[208:211], v[164:167], a[112:115]\n"
    "v_exp_f32 v151, v151\n"
    "v_mfma_f32_16x16x32_bf16 a[116:119], v[212:215], v[164:167], a[116:119]\n"
    "v_cvt_pk_bf16_f32 v171, v150, v151\n"
    "v_mfma_f32_16x16x32_bf16 a[120:123], v[216:219], v[164:167], a[120:123]\n"
    "v_mfma_f32_16x16x32_bf16 a[124:127], v[220:223], v[164:167], a[124:127]\n"
    "s_add_u32 s67, s63, s59\n"
    "v_mfma_f32_16x16x32_bf16 a[172:175], a[180:183], v[164:167], a[172:175]\n"
    "s_mul_i32 s67, s67, 40960\n"
    "v_mfma_f32_16x16x32_bf16 a[128:131], v[192:195], v[168:171], a[128:131]\n"
    "s_mul_i32 s68, s60, 20480\n"
    "v_mfma_f32_16x16x32_bf16 a[132:135], v[196:199], v[168:171], a[132:135]\n"
    "s_add_u32 s67, s67, s68\n"
    "v_mfma_f32_16x16x32_bf16 a[136:139], v[200:203], v[168:171], a[136:139]\n"
    "v_mfma_f32_16x16x32_bf16 a[140:143], v[204:207], v[168:171], a[140:143]\n"
    "s_mul_i32 s64, s58, 20480\n"
    "v_mfma_f32_16x16x32_bf16 a[144:147], v[208:211], v[168:171], a[144:147]\n"
    "s_add_u32 s64, s64, 49152\n"
    "v_mfma_f32_16x16x32_bf16 a[148:151], v[212:215], v[168:171], a[148:151]\n"
    "v_add_u32 v249, s64, v234\n"
    "v_mfma_f32_16x16x32_bf16 a[152:155], v[216:219], v[168:171], a[152:155]\n"
    "v_mov_b32 v247, s84\n"
    "v_mfma_f32_16x16x32_bf16 a[156:159], v[220:223], v[168:171], a[156:159]\n"
    "v_mfma_f32_16x16x32_bf16 a[176:179], a[180:183], v[168:171], a[176:179]\n"
    "v_accvgpr_read_b32 v27, a160\n"
    "v_accvgpr_read_b32 v28, a164\n"
    "v_accvgpr_read_b32 v29, a168\n"
    "v_accvgpr_read_b32 v30, a172\n"
    "s_nop 3\n"
    "v_accvgpr_read_b32 v31, a176\n"
    "v_cmp_nle_f32 s[88:89], v27, s84\n"
    "v_rcp_f32 v27, v27\n"
    "v_cmp_nle_f32 vcc, v28, v247\n"
    "s_nop 1\n"
    "s_or_b64 s[88:89], s[88:89], vcc\n"
    "v_rcp_f32 v28, v28\n"
    "v_cmp_nle_f32 vcc, v29, v247\n"
    "s_nop 1\n"
    "s_or_b64 s[88:89], s[88:89], vcc\n"
    "v_rcp_f32 v29, v29\n"
    "v_cmp_nle_f32 vcc, v30, v247\n"
    "s_nop 1\n"
    "s_or_b64 s[88:89], s[88:89], vcc\n"
    "v_rcp_f32 v30, v30\n"
    "v_cmp_nle_f32 vcc, v31, v247\n"
    "s_nop 1\n"
    "s_or_b64 s[88:89], s[88:89], vcc\n"
    "v_rcp_f32 v31, v31\n"
    "v_add_u32 v232, 0x20000, v232\n"
    "v_add_u32 v233, 0x20000, v233\n"
    "s_waitcnt vmcnt(4)\n"
    "ds_read_b64_tr_b16 a[192:193], v232 offset:0\n"
    "ds_read_b64_tr_b16 a[194:195], v232 offset:4096\n"
    "ds_read_b64_tr_b16 a[196:197], v233 offset:0\n"
    "ds_read_b64_tr_b16 a[198:199], v233 offset:4096\n"
    "ds_read_b64_tr_b16 a[200:201], v232 offset:512\n"
    "ds_read_b64_tr_b16 a[202:203], v232 offset:4608\n"
    "ds_read_b64_tr_b16 a[204:205], v233 offset:512\n"
    "ds_read_b64_tr_b16 a[206:207], v233 offset:4608\n"
    "ds_read_b64_tr_b16 a[208:209], v232 offset:1024\n"
    "ds_read_b64_tr_b16 a[210:211], v232 offset:5120\n"
    "ds_read_b64_tr_b16 a[212:213], v233 offset:1024\n"
    "ds_read_b64_tr_b16 a[214:215], v233 offset:5120\n"
    "ds_read_b64_tr_b16 a[216:217], v232 offset:1536\n"
    "ds_read_b64_tr_b16 a[218:219], v232 offset:5632\n"
    "ds_read_b64_tr_b16 a[220:221], v233 offset:1536\n"
    "ds_read_b64_tr_b16 a[222:223], v233 offset:5632\n"
    "s_waitcnt lgkmcnt(0)\n"
    "s_barrier\n"
    "v_accvgpr_read_b32 v192, a0\n"
    "v_accvgpr_read_b32 v193, a1\n"
    "v_accvgpr_read_b32 v194, a2\n"
    "v_accvgpr_read_b32 v195, a3\n"
    "v_accvgpr_read_b32 v196, a4\n"
    "v_accvgpr_read_b32 v197, a5\n"
    "v_accvgpr_read_b32 v198, a6\n"
    "v_accvgpr_read_b32 v199, a7\n"
    "v_mul_f32 v192, v192, v27\n"
    "v_mul_f32 v193, v193, v27\n"
    "v_mul_f32 v194, v194, v27\n"
    "v_mul_f32 v195, v195, v27\n"
    "v_mul_f32 v196, v196, v27\n"
    "v_mul_f32 v197, v197, v27\n"
    "v_mul_f32 v198, v198, v27\n"
    "v_mul_f32 v199, v199, v27\n"
    "v_cvt_pk_bf16_f32 v112, v192, v193\n"
    "v_cvt_pk_bf16_f32 v113, v194, v195\n"
    "v_cvt_pk_bf16_f32 v114, v196, v197\n"
    "v_cvt_pk_bf16_f32 v115, v198, v199\n"
    "buffer_store_dwordx4 v[112:115], v234, s[44:47], s67 offen offset:0 sc1\n"
    "ds_write_b128 v249, v[112:115] offset:0\n"
    "v_accvgpr_read_b32 v200, a8\n"
    "v_accvgpr_read_b32 v201, a9\n"
    "v_accvgpr_read_b32 v202, a10\n"
    "v_accvgpr_read_b32 v203, a11\n"
    "v_accvgpr_read_b32 v204, a12\n"
    "v_accvgpr_read_b32 v205, a13\n"
    "v_accvgpr_read_b32 v206, a14\n"
    "v_accvgpr_read_b32 v207, a15\n"
    "v_mul_f32 v200, v200, v27\n"
    "v_mul_f32 v201, v201, v27\n"
    "v_mul_f32 v202, v202, v27\n"
    "v_mul_f32 v203, v203, v27\n"
    "v_mul_f32 v204, v204, v27\n"
    "v_mul_f32 v205, v205, v27\n"
    "v_mul_f32 v206, v206, v27\n"
    "v_mul_f32 v207, v207, v27\n"
    "v_cvt_pk_bf16_f32 v116, v200, v201\n"
    "v_cvt_pk_bf16_f32 v117, v202, v203\n"
    "v_cvt_pk_bf16_f32 v118, v204, v205\n"
    "v_cvt_pk_bf16_f32 v119, v206, v207\n"
    "buffer_store_dwordx4 v[116:119], v234, s[44:47], s67 offen offset:1024 sc1\n"
    "ds_write_b128 v249, v[116:119] offset:1024\n"
    "v_accvgpr_read_b32 v208, a16\n"
    "v_accvgpr_read_b32 v209, a17\n"
    "v_accvgpr_read_b32 v210, a18\n"
    "v_accvgpr_read_b32 v211, a19\n"
    "v_accvgpr_read_b32 v212, a20\n"
    "v_accvgpr_read_b32 v213, a21\n"
    "v_accvgpr_read_b32 v214, a22\n"
    "v_accvgpr_read_b32 v215, a23\n"
    "v_mul_f32 v208, v208, v27\n"
    "v_mul_f32 v209, v209, v27\n"
    "v_mul_f32 v210, v210, v27\n"
    "v_mul_f32 v211, v211, v27\n"
    "v_mul_f32 v212, v212, v27\n"
    "v_mul_f32 v213, v213, v27\n"
    "v_mul_f32 v214, v214, v27\n"
    "v_mul_f32 v215, v215, v27\n"
    "v_cvt_pk_bf16_f32 v120, v208, v209\n"
    "v_cvt_pk_bf16_f32 v121, v210, v211\n"
    "v_cvt_pk_bf16_f32 v122, v212, v213\n"
    "v_cvt_pk_bf16_f32 v123, v214, v215\n"
    "buffer_store_dwordx4 v[120:123], v234, s[44:47], s67 offen offset:2048 sc1\n"
    "ds_write_b128 v249, v[120:123] offset:2048\n"
    "v_accvgpr_read_b32 v216, a24\n"
    "v_accvgpr_read_b32 v217, a25\n"
    "v_accvgpr_read_b32 v218, a26\n"
    "v_accvgpr_read_b32 v219, a27\n"
    "v_accvgpr_read_b32 v220, a28\n"
    "v_accvgpr_read_b32 v221, a29\n"
    "v_accvgpr_read_b32 v222, a30\n"
    "v_accvgpr_read_b32 v223, a31\n"
    "v_mul_f32 v216, v216, v27\n"
    "v_mul_f32 v217, v217, v27\n"
    "v_mul_f32 v218, v218, v27\n"
    "v_mul_f32 v219, v219, v27\n"
    "v_mul_f32 v220, v220, v27\n"
    "v_mul_f32 v221, v221, v27\n"
    "v_mul_f32 v222, v222, v27\n"
    "v_mul_f32 v223, v223, v27\n"
    "v_cvt_pk_bf16_f32 v124, v216, v217\n"
    "v_cvt_pk_bf16_f32 v125, v218, v219\n"
    "v_cvt_pk_bf16_f32 v126, v220, v221\n"
    "v_cvt_pk_bf16_f32 v127, v222, v223\n"
    "buffer_store_dwordx4 v[124:127], v234, s[44:47], s67 offen offset:3072 sc1\n"
    "ds_write_b128 v249, v[124:127] offset:3072\n"
    "s_add_u32 s67, s67, 4096\n"
    "s_waitcnt lgkmcnt(4)\n"
    "v_mfma_f32_16x16x32_bf16 a[0:3], a[192:195], v[112:115], 0\n"
    "v_accvgpr_read_b32 v192, a32\n"
    "v_accvgpr_read_b32 v193, a33\n"
    "v_accvgpr_read_b32 v194, a34\n"
    "v_accvgpr_read_b32 v195, a35\n"
    "v_accvgpr_read_b32 v196, a36\n"
    "v_accvgpr_read_b32 v197, a37\n"
    "v_accvgpr_read_b32 v198, a38\n"
    "v_accvgpr_read_b32 v199, a39\n"
    "v_mul_f32 v192, v192, v28\n"
    "v_mul_f32 v193, v193, v28\n"
    "v_mul_f32 v194, v194, v28\n"
    "v_mul_f32 v195, v195, v28\n"
    "v_add_u32 v232, 0x4000, v232\n"
    "v_add_u32 v233, 0x4000, v233\n"
    "ds_read_b64_tr_b16 a[224:225], v232 offset:0\n"
    "v_mfma_f32_16x16x32_bf16 a[4:7], a[196:199], v[112:115], 0\n"
    "v_mul_f32 v196, v196, v28\n"
    "v_mul_f32 v197, v197, v28\n"
    "v_mul_f32 v198, v198, v28\n"
    "v_mul_f32 v199, v199, v28\n"
    "v_cvt_pk_bf16_f32 v128, v192, v193\n"
    "v_cvt_pk_bf16_f32 v129, v194, v195\n"
    "v_cvt_pk_bf16_f32 v130, v196, v197\n"
    "v_cvt_pk_bf16_f32 v131, v198, v199\n"
    "buffer_store_dwordx4 v[128:131], v234, s[44:47], s67 offen offset:0 sc1\n"
    "ds_write_b128 v249, v[128:131] offset:4096\n"
    "v_accvgpr_read_b32 v200, a40\n"
    "ds_read_b64_tr_b16 a[226:227], v232 offset:4096\n"
    "ds_read_b64_tr_b16 a[228:229], v233 offset:0\n"
    "v_mfma_f32_16x16x32_bf16 a[8:11], a[200:203], v[112:115], 0\n"
    "v_accvgpr_read_b32 v201, a41\n"
    "v_accvgpr_read_b32 v202, a42\n"
    "v_accvgpr_read_b32 v203, a43\n"
    "v_accvgpr_read_b32 v204, a44\n"
    "v_accvgpr_read_b32 v205, a45\n"
    "v_accvgpr_read_b32 v206, a46\n"
    "v_accvgpr_read_b32 v207, a47\n"
    "v_mul_f32 v200, v200, v28\n"
    "v_mul_f32 v201, v201, v28\n"
    "v_mul_f32 v202, v202, v28\n"
    "v_mul_f32 v203, v203, v28\n"
    "ds_read_b64_tr_b16 a[230:231], v233 offset:4096\n"
    "ds_read_b64_tr_b16 a[232:233], v232 offset:512\n"
    "v_mfma_f32_16x16x32_bf16 a[12:15], a[204:207], v[112:115], 0\n"
    "v_mul_f32 v204, v204, v28\n"
    "v_mul_f32 v205, v205, v28\n"
    "v_mul_f32 v206, v206, v28\n"
    "v_mul_f32 v207, v207, v28\n"
    "v_cvt_pk_bf16_f32 v132, v200, v201\n"
    "v_cvt_pk_bf16_f32 v133, v202, v203\n"
    "v_cvt_pk_bf16_f32 v134, v204, v205\n"
    "v_cvt_pk_bf16_f32 v135, v206, v207\n"
    "buffer_store_dwordx4 v[132:135], v234, s[44:47], s67 offen offset:1024 sc1\n"
    "ds_write_b128 v249, v[132:135] offset:5120\n"
    "v_accvgpr_read_b32 v208, a48\n"
    "ds_read_b64_tr_b16 a[234:235], v232 offset:4608\n"
    "ds_read_b64_tr_b16 a[236:237], v233 offset:512\n"
    "v_mfma_f32_16x16x32_bf16 a[16:19], a[208:211], v[112:115], 0\n"
    "v_accvgpr_read_b32 v209, a49\n"
    "v_accvgpr_read_b32 v210, a50\n"
    "v_accvgpr_read_b32 v211, a51\n"
    "v_accvgpr_read_b32 v212, a52\n"
    "v_accvgpr_read_b32 v213, a53\n"
    "v_accvgpr_read_b32 v214, a54\n"
    "v_accvgpr_read_b32 v215, a55\n"
    "v_mul_f32 v208, v208, v28\n"
    "v_mul_f32 v209, v209, v28\n"
    "v_mul_f32 v210, v210, v28\n"
    "v_mul_f32 v211, v211, v28\n"
    "ds_read_b64_tr_b16 a[238:239], v233 offset:4608\n"
    "ds_read_b64_tr_b16 a[240:241], v232 offset:1024\n"
    "ds_read_b64_tr_b16 a[242:243], v232 offset:5120\n"
    "v_mfma_f32_16x16x32_bf16 a[20:23], a[212:215], v[112:115], 0\n"
    "v_mul_f32 v212, v212, v28\n"
    "v_mul_f32 v213, v213, v28\n"
    "v_mul_f32 v214, v214, v28\n"
    "v_mul_f32 v215, v215, v28\n"
    "v_cvt_pk_bf16_f32 v136, v208, v209\n"
    "v_cvt_pk_bf16_f32 v137, v210, v211\n"
    "v_cvt_pk_bf16_f32 v138, v212, v213\n"
    "v_cvt_pk_bf16_f32 v139, v214, v215\n"
    "buffer_store_dwordx4 v[136:139], v234, s[44:47], s67 offen offset:2048 sc1\n"
    "ds_write_b128 v249, v[136:139] offset:6144\n"
    "v_accvgpr_read_b32 v216, a56\n"
    "ds_read_b64_tr_b16 a[244:245], v233 offset:1024\n"
    "ds_read_b64_tr_b16 a[246:247], v233 offset:5120\n"
    "v_mfma_f32_16x16x32_bf16 a[24:27], a[216:219], v[112:115], 0\n"
    "v_accvgpr_read_b32 v217, a57\n"
    "v_accvgpr_read_b32 v218, a58\n"
    "v_accvgpr_read_b32 v219, a59\n"
    "v_accvgpr_read_b32 v220, a60\n"
    "v_accvgpr_read_b32 v221, a61\n"
    "v_accvgpr_read_b32 v222, a62\n"
    "v_accvgpr_read_b32 v223, a63\n"
    "v_mul_f32 v216, v216, v28\n"
    "v_mul_f32 v217, v217, v28\n"
    "v_mul_f32 v218, v218, v28\n"
    "v_mul_f32 v219, v219, v28\n"
    "ds_read_b64_tr_b16 a[248:249], v232 offset:1536\n"
    "ds_read_b64_tr_b16 a[250:251], v232 offset:5632\n"
    "v_mfma_f32_16x16x32_bf16 a[28:31], a[220:223], v[112:115], 0\n"
    "v_mul_f32 v220, v220, v28\n"
    "v_mul_f32 v221, v221, v28\n"
    "v_mul_f32 v222, v222, v28\n"
    "v_mul_f32 v223, v223, v28\n"
    "v_cvt_pk_bf16_f32 v140, v216, v217\n"
    "v_cvt_pk_bf16_f32 v141, v218, v219\n"
    "v_cvt_pk_bf16_f32 v142, v220, v221\n"
    "v_cvt_pk_bf16_f32 v143, v222, v223\n"
    "buffer_store_dwordx4 v[140:143], v234, s[44:47], s67 offen offset:3072 sc1\n"
    "ds_write_b128 v249, v[140:143] offset:7168\n"
    "s_add_u32 s67, s67, 4096\n"
    "ds_read_b64_tr_b16 a[252:253], v233 offset:1536\n"
    "ds_read_b64_tr_b16 a[254:255], v233 offset:5632\n"
    "v_mfma_f32_16x16x32_bf16 a[32:35], a[192:195], v[128:131], 0\n"
    "v_accvgpr_read_b32 v192, a64\n"
    "v_accvgpr_read_b32 v193, a65\n"
    "v_accvgpr_read_b32 v194, a66\n"
    "v_accvgpr_read_b32 v195, a67\n"
    "v_accvgpr_read_b32 v196, a68\n"
    "v_accvgpr_read_b32 v197, a69\n"
    "v_accvgpr_read_b32 v198, a70\n"
    "v_accvgpr_read_b32 v199, a71\n"
    "v_mul_f32 v192, v192, v29\n"
    "v_mul_f32 v193, v193, v29\n"
    "v_mul_f32 v194, v194, v29\n"
    "v_mul_f32 v195, v195, v29\n"
    "v_mfma_f32_16x16x32_bf16 a[36:39], a[196:199], v[128:131], 0\n"
    "v_mul_f32 v196, v196, v29\n"
    "v_mul_f32 v197, v197, v29\n"
    "v_mul_f32 v198, v198, v29\n"
    "v_mul_f32 v199, v199, v29\n"
    "v_cvt_pk_bf16_f32 v144, v192, v193\n"
    "v_cvt_pk_bf16_f32 v145, v194, v195\n"
    "v_cvt_pk_bf16_f32 v146, v196, v197\n"
    "v_cvt_pk_bf16_f32 v147, v198, v199\n"
    "buffer_store_dwordx4 v[144:147], v234, s[44:47], s67 offen offset:0 sc1\n"
    "ds_write_b128 v249, v[144:147] offset:8192\n"
    "v_accvgpr_read_b32 v200, a72\n"
    "v_mfma_f32_16x16x32_bf16 a[40:43], a[200:203], v[128:131], 0\n"
    "v_accvgpr_read_b32 v201, a73\n"
    "v_accvgpr_read_b32 v202, a74\n"
    "v_accvgpr_read_b32 v203, a75\n"
    "v_accvgpr_read_b32 v204, a76\n"
    "v_accvgpr_read_b32 v205, a77\n"
    "v_accvgpr_read_b32 v206, a78\n"
    "v_accvgpr_read_b32 v207, a79\n"
    "v_mul_f32 v200, v200, v29\n"
    "v_mul_f32 v201, v201, v29\n"
    "v_mul_f32 v202, v202, v29\n"
    "v_mul_f32 v203, v203, v29\n"
    "v_mfma_f32_16x16x32_bf16 a[44:47], a[204:207], v[128:131], 0\n"
    "v_mul_f32 v204, v204, v29\n"
    "v_mul_f32 v205, v205, v29\n"
    "v_mul_f32 v206, v206, v29\n"
    "v_mul_f32 v207, v207, v29\n"
    "v_cvt_pk_bf16_f32 v148, v200, v201\n"
    "v_cvt_pk_bf16_f32 v149, v202, v203\n"
    "v_cvt_pk_bf16_f32 v150, v204, v205\n"
    "v_cvt_pk_bf16_f32 v151, v206, v207\n"
    "buffer_store_dwordx4 v[148:151], v234, s[44:47], s67 offen offset:1024 sc1\n"
    "ds_write_b128 v249, v[148:151] offset:9216\n"
    "v_accvgpr_read_b32 v208, a80\n"
    "v_mfma_f32_16x16x32_bf16 a[48:51], a[208:211], v[128:131], 0\n"
    "v_accvgpr_read_b32 v209, a81\n"
    "v_accvgpr_read_b32 v210, a82\n"
    "v_accvgpr_read_b32 v211, a83\n"
    "v_accvgpr_read_b32 v212, a84\n"
    "v_accvgpr_read_b32 v213, a85\n"
    "v_accvgpr_read_b32 v214, a86\n"
    "v_accvgpr_read_b32 v215, a87\n"
    "v_mul_f32 v208, v208, v29\n"
    "v_mul_f32 v209, v209, v29\n"
    "v_mul_f32 v210, v210, v29\n"
    "v_mul_f32 v211, v211, v29\n"
    "v_mfma_f32_16x16x32_bf16 a[52:55], a[212:215], v[128:131], 0\n"
    "v_mul_f32 v212, v212, v29\n"
    "v_mul_f32 v213, v213, v29\n"
    "v_mul_f32 v214, v214, v29\n"
    "v_mul_f32 v215, v215, v29\n"
    "v_cvt_pk_bf16_f32 v152, v208, v209\n"
    "v_cvt_pk_bf16_f32 v153, v210, v211\n"
    "v_cvt_pk_bf16_f32 v154, v212, v213\n"
    "v_cvt_pk_bf16_f32 v155, v214, v215\n"
    "buffer_store_dwordx4 v[152:155], v234, s[44:47], s67 offen offset:2048 sc1\n"
    "ds_write_b128 v249, v[152:155] offset:10240\n"
    "v_accvgpr_read_b32 v216, a88\n"
    "v_mfma_f32_16x16x32_bf16 a[56:59], a[216:219], v[128:131], 0\n"
    "v_accvgpr_read_b32 v217, a89\n"
    "v_accvgpr_read_b32 v218, a90\n"
    "v_accvgpr_read_b32 v219, a91\n"
    "v_accvgpr_read_b32 v220, a92\n"
    "v_accvgpr_read_b32 v221, a93\n"
    "v_accvgpr_read_b32 v222, a94\n"
    "v_accvgpr_read_b32 v223, a95\n"
    "v_mul_f32 v216, v216, v29\n"
    "v_mul_f32 v217, v217, v29\n"
    "v_mul_f32 v218, v218, v29\n"
    "v_mul_f32 v219, v219, v29\n"
    "v_mfma_f32_16x16x32_bf16 a[60:63], a[220:223], v[128:131], 0\n"
    "v_mul_f32 v220, v220, v29\n"
    "v_mul_f32 v221, v221, v29\n"
    "v_mul_f32 v222, v222, v29\n"
    "v_mul_f32 v223, v223, v29\n"
    "v_cvt_pk_bf16_f32 v156, v216, v217\n"
    "v_cvt_pk_bf16_f32 v157, v218, v219\n"
    "v_cvt_pk_bf16_f32 v158, v220, v221\n"
    "v_cvt_pk_bf16_f32 v159, v222, v223\n"
    "buffer_store_dwordx4 v[156:159], v234, s[44:47], s67 offen offset:3072 sc1\n"
    "ds_write_b128 v249, v[156:159] offset:11264\n"
    "s_add_u32 s67, s67, 4096\n"
    "v_mfma_f32_16x16x32_bf16 a[64:67], a[192:195], v[144:147], 0\n"
    "v_accvgpr_read_b32 v192, a96\n"
    "v_accvgpr_read_b32 v193, a97\n"
    "v_accvgpr_read_b32 v194, a98\n"
    "v_accvgpr_read_b32 v195, a99\n"
    "v_accvgpr_read_b32 v196, a100\n"
    "v_accvgpr_read_b32 v197, a101\n"
    "v_accvgpr_read_b32 v198, a102\n"
    "v_accvgpr_read_b32 v199, a103\n"
    "v_mul_f32 v192, v192, v30\n"
    "v_mul_f32 v193, v193, v30\n"
    "v_mul_f32 v194, v194, v30\n"
    "v_mul_f32 v195, v195, v30\n"
    "v_mfma_f32_16x16x32_bf16 a[68:71], a[196:199], v[144:147], 0\n"
    "v_mul_f32 v196, v196, v30\n"
    "v_mul_f32 v197, v197, v30\n"
    "v_mul_f32 v198, v198, v30\n"
    "v_mul_f32 v199, v199, v30\n"
    "v_cvt_pk_bf16_f32 v160, v192, v193\n"
    "v_cvt_pk_bf16_f32 v161, v194, v195\n"
    "v_cvt_pk_bf16_f32 v162, v196, v197\n"
    "v_cvt_pk_bf16_f32 v163, v198, v199\n"
    "buffer_store_dwordx4 v[160:163], v234, s[44:47], s67 offen offset:0 sc1\n"
    "ds_write_b128 v249, v[160:163] offset:12288\n"
    "v_accvgpr_read_b32 v200, a104\n"
    "v_mfma_f32_16x16x32_bf16 a[72:75], a[200:203], v[144:147], 0\n"
    "v_accvgpr_read_b32 v201, a105\n"
    "v_accvgpr_read_b32 v202, a106\n"
    "v_accvgpr_read_b32 v203, a107\n"
    "v_accvgpr_read_b32 v204, a108\n"
    "v_accvgpr_read_b32 v205, a109\n"
    "v_accvgpr_read_b32 v206, a110\n"
    "v_accvgpr_read_b32 v207, a111\n"
    "v_mul_f32 v200, v200, v30\n"
    "v_mul_f32 v201, v201, v30\n"
    "v_mul_f32 v202, v202, v30\n"
    "v_mul_f32 v203, v203, v30\n"
    "v_mfma_f32_16x16x32_bf16 a[76:79], a[204:207], v[144:147], 0\n"
    "v_mul_f32 v204, v204, v30\n"
    "v_mul_f32 v205, v205, v30\n"
    "v_mul_f32 v206, v206, v30\n"
    "v_mul_f32 v207, v207, v30\n"
    "v_cvt_pk_bf16_f32 v164, v200, v201\n"
    "v_cvt_pk_bf16_f32 v165, v202, v203\n"
    "v_cvt_pk_bf16_f32 v166, v204, v205\n"
    "v_cvt_pk_bf16_f32 v167, v206, v207\n"
    "buffer_store_dwordx4 v[164:167], v234, s[44:47], s67 offen offset:1024 sc1\n"
    "ds_write_b128 v249, v[164:167] offset:13312\n"
    "v_accvgpr_read_b32 v208, a112\n"
    "v_mfma_f32_16x16x32_bf16 a[80:83], a[208:211], v[144:147], 0\n"
    "v_accvgpr_read_b32 v209, a113\n"
    "v_accvgpr_read_b32 v210, a114\n"
    "v_accvgpr_read_b32 v211, a115\n"
    "v_accvgpr_read_b32 v212, a116\n"
    "v_accvgpr_read_b32 v213, a117\n"
    "v_accvgpr_read_b32 v214, a118\n"
    "v_accvgpr_read_b32 v215, a119\n"
    "v_mul_f32 v208, v208, v30\n"
    "v_mul_f32 v209, v209, v30\n"
    "v_mul_f32 v210, v210, v30\n"
    "v_mul_f32 v211, v211, v30\n"
    "v_mfma_f32_16x16x32_bf16 a[84:87], a[212:215], v[144:147], 0\n"
    "v_mul_f32 v212, v212, v30\n"
    "v_mul_f32 v213, v213, v30\n"
    "v_mul_f32 v214, v214, v30\n"
    "v_mul_f32 v215, v215, v30\n"
    "v_cvt_pk_bf16_f32 v168, v208, v209\n"
    "v_cvt_pk_bf16_f32 v169, v210, v211\n"
    "v_cvt_pk_bf16_f32 v170, v212, v213\n"
    "v_cvt_pk_bf16_f32 v171, v214, v215\n"
    "buffer_store_dwordx4 v[168:171], v234, s[44:47], s67 offen offset:2048 sc1\n"
    "ds_write_b128 v249, v[168:171] offset:14336\n"
    "v_accvgpr_read_b32 v216, a120\n"
    "v_mfma_f32_16x16x32_bf16 a[88:91], a[216:219], v[144:147], 0\n"
    "v_accvgpr_read_b32 v217, a121\n"
    "v_accvgpr_read_b32 v218, a122\n"
    "v_accvgpr_read_b32 v219, a123\n"
    "v_accvgpr_read_b32 v220, a124\n"
    "v_accvgpr_read_b32 v221, a125\n"
    "v_accvgpr_read_b32 v222, a126\n"
    "v_accvgpr_read_b32 v223, a127\n"
    "v_mul_f32 v216, v216, v30\n"
    "v_mul_f32 v217, v217, v30\n"
    "v_mul_f32 v218, v218, v30\n"
    "v_mul_f32 v219, v219, v30\n"
    "v_mfma_f32_16x16x32_bf16 a[92:95], a[220:223], v[144:147], 0\n"
    "v_mul_f32 v220, v220, v30\n"
    "v_mul_f32 v221, v221, v30\n"
    "v_mul_f32 v222, v222, v30\n"
    "v_mul_f32 v223, v223, v30\n"
    "v_cvt_pk_bf16_f32 v172, v216, v217\n"
    "v_cvt_pk_bf16_f32 v173, v218, v219\n"
    "v_cvt_pk_bf16_f32 v174, v220, v221\n"
    "v_cvt_pk_bf16_f32 v175, v222, v223\n"
    "buffer_store_dwordx4 v[172:175], v234, s[44:47], s67 offen offset:3072 sc1\n"
    "ds_write_b128 v249, v[172:175] offset:15360\n"
    "s_add_u32 s67, s67, 4096\n"
    "v_mfma_f32_16x16x32_bf16 a[96:99], a[192:195], v[160:163], 0\n"
    "v_accvgpr_read_b32 v192, a128\n"
    "v_accvgpr_read_b32 v193, a129\n"
    "v_accvgpr_read_b32 v194, a130\n"
    "v_accvgpr_read_b32 v195, a131\n"
    "v_accvgpr_read_b32 v196, a132\n"
    "v_accvgpr_read_b32 v197, a133\n"
    "v_accvgpr_read_b32 v198, a134\n"
    "v_accvgpr_read_b32 v199, a135\n"
    "v_mul_f32 v192, v192, v31\n"
    "v_mul_f32 v193, v193, v31\n"
    "v_mul_f32 v194, v194, v31\n"
    "v_mul_f32 v195, v195, v31\n"
    "v_mfma_f32_16x16x32_bf16 a[100:103], a[196:199], v[160:163], 0\n"
    "v_mul_f32 v196, v196, v31\n"
    "v_mul_f32 v197, v197, v31\n"
    "v_mul_f32 v198, v198, v31\n"
    "v_mul_f32 v199, v199, v31\n"
    "v_cvt_pk_bf16_f32 v176, v192, v193\n"
    "v_cvt_pk_bf16_f32 v177, v194, v195\n"
    "v_cvt_pk_bf16_f32 v178, v196, v197\n"
    "v_cvt_pk_bf16_f32 v179, v198, v199\n"
    "buffer_store_dwordx4 v[176:179], v234, s[44:47], s67 offen offset:0 sc1\n"
    "ds_write_b128 v249, v[176:179] offset:16384\n"
    "v_accvgpr_read_b32 v200, a136\n"
    "v_mfma_f32_16x16x32_bf16 a[104:107], a[200:203], v[160:163], 0\n"
    "v_accvgpr_read_b32 v201, a137\n"
    "v_accvgpr_read_b32 v202, a138\n"
    "v_accvgpr_read_b32 v203, a139\n"
    "v_accvgpr_read_b32 v204, a140\n"
    "v_accvgpr_read_b32 v205, a141\n"
    "v_accvgpr_read_b32 v206, a142\n"
    "v_accvgpr_read_b32 v207, a143\n"
    "v_mul_f32 v200, v200, v31\n"
    "v_mul_f32 v201, v201, v31\n"
    "v_mul_f32 v202, v202, v31\n"
    "v_mul_f32 v203, v203, v31\n"
    "v_mfma_f32_16x16x32_bf16 a[108:111], a[204:207], v[160:163], 0\n"
    "v_mul_f32 v204, v204, v31\n"
    "v_mul_f32 v205, v205, v31\n"
    "v_mul_f32 v206, v206, v31\n"
    "v_mul_f32 v207, v207, v31\n"
    "v_cvt_pk_bf16_f32 v180, v200, v201\n"
    "v_cvt_pk_bf16_f32 v181, v202, v203\n"
    "v_cvt_pk_bf16_f32 v182, v204, v205\n"
    "v_cvt_pk_bf16_f32 v183, v206, v207\n"
    "buffer_store_dwordx4 v[180:183], v234, s[44:47], s67 offen offset:1024 sc1\n"
    "ds_write_b128 v249, v[180:183] offset:17408\n"
    "v_accvgpr_read_b32 v208, a144\n"
    "v_mfma_f32_16x16x32_bf16 a[112:115], a[208:211], v[160:163], 0\n"
    "v_accvgpr_read_b32 v209, a145\n"
    "v_accvgpr_read_b32 v210, a146\n"
    "v_accvgpr_read_b32 v211, a147\n"
    "v_accvgpr_read_b32 v212, a148\n"
    "v_accvgpr_read_b32 v213, a149\n"
    "v_accvgpr_read_b32 v214, a150\n"
    "v_accvgpr_read_b32 v215, a151\n"
    "v_mul_f32 v208, v208, v31\n"
    "v_mul_f32 v209, v209, v31\n"
    "v_mul_f32 v210, v210, v31\n"
    "v_mul_f32 v211, v211, v31\n"
    "v_mfma_f32_16x16x32_bf16 a[116:119], a[212:215], v[160:163], 0\n"
    "v_mul_f32 v212, v212, v31\n"
    "v_mul_f32 v213, v213, v31\n"
    "v_mul_f32 v214, v214, v31\n"
    "v_mul_f32 v215, v215, v31\n"
    "v_cvt_pk_bf16_f32 v184, v208, v209\n"
    "v_cvt_pk_bf16_f32 v185, v210, v211\n"
    "v_cvt_pk_bf16_f32 v186, v212, v213\n"
    "v_cvt_pk_bf16_f32 v187, v214, v215\n"
    "buffer_store_dwordx4 v[184:187], v234, s[44:47], s67 offen offset:2048 sc1\n"
    "ds_write_b128 v249, v[184:187] offset:18432\n"
    "v_accvgpr_read_b32 v216, a152\n"
    "v_mfma_f32_16x16x32_bf16 a[120:123], a[216:219], v[160:163], 0\n"
    "v_accvgpr_read_b32 v217, a153\n"
    "v_accvgpr_read_b32 v218, a154\n"
    "v_accvgpr_read_b32 v219, a155\n"
    "v_accvgpr_read_b32 v220, a156\n"
    "v_accvgpr_read_b32 v221, a157\n"
    "v_accvgpr_read_b32 v222, a158\n"
    "v_accvgpr_read_b32 v223, a159\n"
    "v_mul_f32 v216, v216, v31\n"
    "v_mul_f32 v217, v217, v31\n"
    "v_mul_f32 v218, v218, v31\n"
    "v_mul_f32 v219, v219, v31\n"
    "v_mfma_f32_16x16x32_bf16 a[124:127], a[220:223], v[160:163], 0\n"
    "v_mul_f32 v220, v220, v31\n"
    "v_mul_f32 v221, v221, v31\n"
    "v_mul_f32 v222, v222, v31\n"
    "v_mul_f32 v223, v223, v31\n"
    "v_cvt_pk_bf16_f32 v188, v216, v217\n"
    "v_cvt_pk_bf16_f32 v189, v218, v219\n"
    "v_cvt_pk_bf16_f32 v190, v220, v221\n"
    "v_cvt_pk_bf16_f32 v191, v222, v223\n"
    "buffer_store_dwordx4 v[188:191], v234, s[44:47], s67 offen offset:3072 sc1\n"
    "ds_write_b128 v249, v[188:191] offset:19456\n"
    "s_add_u32 s67, s67, 4096\n"
    "v_mfma_f32_16x16x32_bf16 a[128:131], a[192:195], v[176:179], 0\n"
    "v_mfma_f32_16x16x32_bf16 a[132:135], a[196:199], v[176:179], 0\n"
    "v_mfma_f32_16x16x32_bf16 a[136:139], a[200:203], v[176:179], 0\n"
    "v_mfma_f32_16x16x32_bf16 a[140:143], a[204:207], v[176:179], 0\n"
    "v_mfma_f32_16x16x32_bf16 a[144:147], a[208:211], v[176:179], 0\n"
    "v_mfma_f32_16x16x32_bf16 a[148:151], a[212:215], v[176:179], 0\n"
    "v_mfma_f32_16x16x32_bf16 a[152:155], a[216:219], v[176:179], 0\n"
    "v_mfma_f32_16x16x32_bf16 a[156:159], a[220:223], v[176:179], 0\n"
    "s_cmp_lg_u64 s[88:89], 0\n"
    "s_cselect_b32 s64, 1, 0\n"
    "v_mov_b32 v244, s64\n"
    "s_lshl_b32 s65, s58, 2\n"
    "s_add_u32 s65, s65, 131072\n"
    "v_mov_b32 v245, s65\n"
    "ds_write_b32 v245, v244\n"
    "s_waitcnt lgkmcnt(0)\n"
    "s_barrier\n"
    "v_mov_b32 v245, 131072\n"
    "ds_read_b128 v[248:251], v245\n"
    "s_xor_b32 s64, s58, 2\n"
    "s_mul_i32 s64, s64, 20480\n"
    "s_add_u32 s64, s64, 49152\n"
    "v_add_u32 v244, s64, v234\n"
    "s_waitcnt lgkmcnt(0)\n"
    "v_or_b32 v248, v248, v249\n"
    "v_or_b32 v250, v250, v251\n"
    "v_or_b32 v248, v248, v250\n"
    "s_nop 0\n"
    "v_readfirstlane_b32 s65, v248\n"
    "s_xor_b32 s66, s85, 1\n"
    "s_nop 0\n"
    "s_and_b32 s65, s65, s66\n"
    "s_cmp_lg_u32 s65, 0\n"
    "s_cbranch_scc1 FALLBACK\n"
    "s_waitcnt lgkmcnt(0)\n"
    "v_mfma_f32_16x16x32_bf16 a[0:3], a[224:227], v[116:119], a[0:3]\n"
    "ds_read_b128 v[32:35], v244 offset:0\n"
    "v_mfma_f32_16x16x32_bf16 a[4:7], a[228:231], v[116:119], a[4:7]\n"
    "ds_read_b128 v[36:39], v244 offset:1024\n"
    "v_mfma_f32_16x16x32_bf16 a[8:11], a[232:235], v[116:119], a[8:11]\n"
    "v_mfma_f32_16x16x32_bf16 a[12:15], a[236:239], v[116:119], a[12:15]\n"
    "ds_read_b128 v[40:43], v244 offset:2048\n"
    "v_mfma_f32_16x16x32_bf16 a[16:19], a[240:243], v[116:119], a[16:19]\n"
    "ds_read_b128 v[44:47], v244 offset:3072\n"
    "v_mfma_f32_16x16x32_bf16 a[20:23], a[244:247], v[116:119], a[20:23]\n"
    "v_mfma_f32_16x16x32_bf16 a[24:27], a[248:251], v[116:119], a[24:27]\n"
    "ds_read_b128 v[48:51], v244 offset:4096\n"
    "v_mfma_f32_16x16x32_bf16 a[28:31], a[252:255], v[116:119], a[28:31]\n"
    "ds_read_b128 v[52:55], v244 offset:5120\n"
    "v_mfma_f32_16x16x32_bf16 a[32:35], a[224:227], v[132:135], a[32:35]\n"
    "v_mfma_f32_16x16x32_bf16 a[36:39], a[228:231], v[132:135], a[36:39]\n"
    "ds_read_b128 v[56:59], v244 offset:6144\n"
    "v_mfma_f32_16x16x32_bf16 a[40:43], a[232:235], v[132:135], a[40:43]\n"
    "ds_read_b128 v[60:63], v244 offset:7168\n"
    "v_mfma_f32_16x16x32_bf16 a[44:47], a[236:239], v[132:135], a[44:47]\n"
    "v_mfma_f32_16x16x32_bf16 a[48:51], a[240:243], v[132:135], a[48:51]\n"
    "ds_read_b128 v[64:67], v244 offset:8192\n"
    "v_mfma_f32_16x16x32_bf16 a[52:55], a[244:247], v[132:135], a[52:55]\n"
    "ds_read_b128 v[68:71], v244 offset:9216\n"
    "v_mfma_f32_16x16x32_bf16 a[56:59], a[248:251], v[132:135], a[56:59]\n"
    "v_mfma_f32_16x16x32_bf16 a[60:63], a[252:255], v[132:135], a[60:63]\n"
    "ds_read_b128 v[72:75], v244 offset:10240\n"
    "v_mfma_f32_16x16x32_bf16 a[64:67], a[224:227], v[148:151], a[64:67]\n"
    "v_mfma_f32_16x16x32_bf16 a[68:71], a[228:231], v[148:151], a[68:71]\n"
    "ds_read_b128 v[76:79], v244 offset:11264\n"
    "v_mfma_f32_16x16x32_bf16 a[72:75], a[232:235], v[148:151], a[72:75]\n"
    "ds_read_b128 v[80:83], v244 offset:12288\n"
    "v_mfma_f32_16x16x32_bf16 a[76:79], a[236:239], v[148:151], a[76:79]\n"
    "v_mfma_f32_16x16x32_bf16 a[80:83], a[240:243], v[148:151], a[80:83]\n"
    "ds_read_b128 v[84:87], v244 offset:13312\n"
    "v_mfma_f32_16x16x32_bf16 a[84:87], a[244:247], v[148:151], a[84:87]\n"
    "ds_read_b128 v[88:91], v244 offset:14336\n"
    "v_mfma_f32_16x16x32_bf16 a[88:91], a[248:251], v[148:151], a[88:91]\n"
    "v_mfma_f32_16x16x32_bf16 a[92:95], a[252:255], v[148:151], a[92:95]\n"
    "ds_read_b128 v[92:95], v244 offset:15360\n"
    "v_mfma_f32_16x16x32_bf16 a[96:99], a[224:227], v[164:167], a[96:99]\n"
    "ds_read_b128 v[96:99], v244 offset:16384\n"
    "v_mfma_f32_16x16x32_bf16 a[100:103], a[228:231], v[164:167], a[100:103]\n"
    "v_mfma_f32_16x16x32_bf16 a[104:107], a[232:235], v[164:167], a[104:107]\n"
    "ds_read_b128 v[100:103], v244 offset:17408\n"
    "v_mfma_f32_16x16x32_bf16 a[108:111], a[236:239], v[164:167], a[108:111]\n"
    "ds_read_b128 v[104:107], v244 offset:18432\n"
    "v_mfma_f32_16x16x32_bf16 a[112:115], a[240:243], v[164:167], a[112:115]\n"
    "v_mfma_f32_16x16x32_bf16 a[116:119], a[244:247], v[164:167], a[116:119]\n"
    "ds_read_b128 v[108:111], v244 offset:19456\n"
    "v_mfma_f32_16x16x32_bf16 a[120:123], a[248:251], v[164:167], a[120:123]\n"
    "v_mfma_f32_16x16x32_bf16 a[124:127], a[252:255], v[164:167], a[124:127]\n"
    "v_add_u32 v232, 0xfffdc000, v232\n"
    "v_mfma_f32_16x16x32_bf16 a[128:131], a[224:227], v[180:183], a[128:131]\n"
    "v_add_u32 v233, 0xfffdc000, v233\n"
    "v_mfma_f32_16x16x32_bf16 a[132:135], a[228:231], v[180:183], a[132:135]\n"
    "s_waitcnt vmcnt(24)\n"
    "s_barrier\n"
    "ds_read_b64_tr_b16 a[192:193], v232 offset:0\n"
    "v_mfma_f32_16x16x32_bf16 a[136:139], a[232:235], v[180:183], a[136:139]\n"
    "ds_read_b64_tr_b16 a[194:195], v232 offset:4096\n"
    "ds_read_b64_tr_b16 a[196:197], v233 offset:0\n"
    "ds_read_b64_tr_b16 a[198:199], v233 offset:4096\n"
    "v_mfma_f32_16x16x32_bf16 a[140:143], a[236:239], v[180:183], a[140:143]\n"
    "ds_read_b64_tr_b16 a[200:201], v232 offset:512\n"
    "ds_read_b64_tr_b16 a[202:203], v232 offset:4608\n"
    "v_mfma_f32_16x16x32_bf16 a[144:147], a[240:243], v[180:183], a[144:147]\n"
    "ds_read_b64_tr_b16 a[204:205], v233 offset:512\n"
    "ds_read_b64_tr_b16 a[206:207], v233 offset:4608\n"
    "ds_read_b64_tr_b16 a[208:209], v232 offset:1024\n"
    "v_mfma_f32_16x16x32_bf16 a[148:151], a[244:247], v[180:183], a[148:151]\n"
    "ds_read_b64_tr_b16 a[210:211], v232 offset:5120\n"
    "ds_read_b64_tr_b16 a[212:213], v233 offset:1024\n"
    "v_mfma_f32_16x16x32_bf16 a[152:155], a[248:251], v[180:183], a[152:155]\n"
    "ds_read_b64_tr_b16 a[214:215], v233 offset:5120\n"
    "ds_read_b64_tr_b16 a[216:217], v232 offset:1536\n"
    "ds_read_b64_tr_b16 a[218:219], v232 offset:5632\n"
    "v_mfma_f32_16x16x32_bf16 a[156:159], a[252:255], v[180:183], a[156:159]\n"
    "ds_read_b64_tr_b16 a[220:221], v233 offset:1536\n"
    "ds_read_b64_tr_b16 a[222:223], v233 offset:5632\n"
    "s_waitcnt lgkmcnt(0)\n"
    "s_waitcnt vmcnt(24)\n"
    "s_waitcnt vmcnt(0)\n"
    "s_waitcnt lgkmcnt(0)\n"
    "s_barrier\n"
    "s_cmp_lg_u32 s58, 0\n"
    "s_cbranch_scc1 D1S_SKIP\n"
    "s_mov_b64 exec, 1\n"
    "s_lshl_b32 s69, s61, 8\n"
    "s_add_u32 s69, s69, 131072\n"
    "v_mov_b32 v250, s69\n"
    "v_mov_b32 v251, s80\n"
    "global_store_dword v250, v251, s[56:57] sc1\n"
    "s_mov_b64 exec, -1\n"
    "D1S_SKIP:\n"
    "v_mfma_f32_16x16x32_bf16 a[0:3], a[192:195], v[120:123], a[0:3]\n"
    "v_add_u32 v232, 0x4000, v232\n"
    "v_mfma_f32_16x16x32_bf16 a[4:7], a[196:199], v[120:123], a[4:7]\n"
    "v_add_u32 v233, 0x4000, v233\n"
    "s_add_u32 s64, s63, s59\n"
    "v_mfma_f32_16x16x32_bf16 a[8:11], a[200:203], v[120:123], a[8:11]\n"
    "s_xor_b32 s64, s64, 1\n"
    "v_mfma_f32_16x16x32_bf16 a[12:15], a[204:207], v[120:123], a[12:15]\n"
    "ds_read_b64_tr_b16 a[224:225], v232 offset:0\n"
    "s_lshl_b32 s64, s64, 17\n"
    "v_mfma_f32_16x16x32_bf16 a[16:19], a[208:211], v[120:123], a[16:19]\n"
    "ds_read_b64_tr_b16 a[226:227], v232 offset:4096\n"
    "s_add_u32 s64, s64, s86\n"
    "v_mfma_f32_16x16x32_bf16 a[20:23], a[212:215], v[120:123], a[20:23]\n"
    "v_mfma_f32_16x16x32_bf16 a[24:27], a[216:219], v[120:123], a[24:27]\n"
    "ds_read_b64_tr_b16 a[228:229], v233 offset:0\n"
    "s_add_u32 s64, s64, 32768\n"
    "v_mfma_f32_16x16x32_bf16 a[28:31], a[220:223], v[120:123], a[28:31]\n"
    "ds_read_b64_tr_b16 a[230:231], v233 offset:4096\n"
    "s_add_u32 m0, s78, 49152\n"
    "v_mfma_f32_16x16x32_bf16 a[32:35], a[192:195], v[136:139], a[32:35]\n"
    "buffer_load_dwordx4 v7, s[40:43], s64 offen offset:0 lds\n"
    "v_mfma_f32_16x16x32_bf16 a[36:39], a[196:199], v[136:139], a[36:39]\n"
    "ds_read_b64_tr_b16 a[232:233], v232 offset:512\n"
    "v_mfma_f32_16x16x32_bf16 a[40:43], a[200:203], v[136:139], a[40:43]\n"
    "ds_read_b64_tr_b16 a[234:235], v232 offset:4608\n"
    "buffer_load_dwordx4 v8, s[40:43], s64 offen offset:1024 lds\n"
    "v_mfma_f32_16x16x32_bf16 a[44:47], a[204:207], v[136:139], a[44:47]\n"
    "buffer_load_dwordx4 v9, s[40:43], s64 offen offset:2048 lds\n"
    "v_mfma_f32_16x16x32_bf16 a[48:51], a[208:211], v[136:139], a[48:51]\n"
    "ds_read_b64_tr_b16 a[236:237], v233 offset:512\n"
    "buffer_load_dwordx4 v10, s[40:43], s64 offen offset:3072 lds\n"
    "v_mfma_f32_16x16x32_bf16 a[52:55], a[212:215], v[136:139], a[52:55]\n"
    "ds_read_b64_tr_b16 a[238:239], v233 offset:4608\n"
    "v_mfma_f32_16x16x32_bf16 a[56:59], a[216:219], v[136:139], a[56:59]\n"
    "s_add_u32 s64, s63, s59\n"
    "v_mfma_f32_16x16x32_bf16 a[60:63], a[220:223], v[136:139], a[60:63]\n"
    "ds_read_b64_tr_b16 a[240:241], v232 offset:1024\n"
    "s_xor_b32 s64, s64, 1\n"
    "v_mfma_f32_16x16x32_bf16 a[64:67], a[192:195], v[152:155], a[64:67]\n"
    "ds_read_b64_tr_b16 a[242:243], v232 offset:5120\n"
    "s_lshl_b32 s64, s64, 17\n"
    "v_mfma_f32_16x16x32_bf16 a[68:71], a[196:199], v[152:155], a[68:71]\n"
    "s_add_u32 s64, s64, s86\n"
    "v_mfma_f32_16x16x32_bf16 a[72:75], a[200:203], v[152:155], a[72:75]\n"
    "ds_read_b64_tr_b16 a[244:245], v233 offset:1024\n"
    "v_mfma_f32_16x16x32_bf16 a[76:79], a[204:207], v[152:155], a[76:79]\n"
    "ds_read_b64_tr_b16 a[246:247], v233 offset:5120\n"
    "s_add_u32 s64, s64, 65536\n"
    "v_mfma_f32_16x16x32_bf16 a[80:83], a[208:211], v[152:155], a[80:83]\n"
    "s_add_u32 m0, s78, 65536\n"
    "v_mfma_f32_16x16x32_bf16 a[84:87], a[212:215], v[152:155], a[84:87]\n"
    "ds_read_b64_tr_b16 a[248:249], v232 offset:1536\n"
    "buffer_load_dwordx4 v7, s[40:43], s64 offen offset:0 lds\n"
    "v_mfma_f32_16x16x32_bf16 a[88:91], a[216:219], v[152:155], a[88:91]\n"
    "ds_read_b64_tr_b16 a[250:251], v232 offset:5632\n"
    "v_mfma_f32_16x16x32_bf16 a[92:95], a[220:223], v[152:155], a[92:95]\n"
    "buffer_load_dwordx4 v8, s[40:43], s64 offen offset:1024 lds\n"
    "v_mfma_f32_16x16x32_bf16 a[96:99], a[192:195], v[168:171], a[96:99]\n"
    "ds_read_b64_tr_b16 a[252:253], v233 offset:1536\n"
    "buffer_load_dwordx4 v9, s[40:43], s64 offen offset:2048 lds\n"
    "v_mfma_f32_16x16x32_bf16 a[100:103], a[196:199], v[168:171], a[100:103]\n"
    "ds_read_b64_tr_b16 a[254:255], v233 offset:5632\n"
    "buffer_load_dwordx4 v10, s[40:43], s64 offen offset:3072 lds\n"
    "v_mfma_f32_16x16x32_bf16 a[104:107], a[200:203], v[168:171], a[104:107]\n"
    "v_mfma_f32_16x16x32_bf16 a[108:111], a[204:207], v[168:171], a[108:111]\n"
    "s_add_u32 s64, s63, s59\n"
    "v_mfma_f32_16x16x32_bf16 a[112:115], a[208:211], v[168:171], a[112:115]\n"
    "s_xor_b32 s64, s64, 1\n"
    "v_mfma_f32_16x16x32_bf16 a[116:119], a[212:215], v[168:171], a[116:119]\n"
    "s_lshl_b32 s64, s64, 17\n"
    "v_mfma_f32_16x16x32_bf16 a[120:123], a[216:219], v[168:171], a[120:123]\n"
    "s_add_u32 s64, s64, s86\n"
    "v_mfma_f32_16x16x32_bf16 a[124:127], a[220:223], v[168:171], a[124:127]\n"
    "v_mfma_f32_16x16x32_bf16 a[128:131], a[192:195], v[184:187], a[128:131]\n"
    "s_add_u32 s64, s64, 98304\n"
    "v_mfma_f32_16x16x32_bf16 a[132:135], a[196:199], v[184:187], a[132:135]\n"
    "s_add_u32 m0, s78, 81920\n"
    "v_mfma_f32_16x16x32_bf16 a[136:139], a[200:203], v[184:187], a[136:139]\n"
    "buffer_load_dwordx4 v7, s[40:43], s64 offen offset:0 lds\n"
    "v_mfma_f32_16x16x32_bf16 a[140:143], a[204:207], v[184:187], a[140:143]\n"
    "v_mfma_f32_16x16x32_bf16 a[144:147], a[208:211], v[184:187], a[144:147]\n"
    "buffer_load_dwordx4 v8, s[40:43], s64 offen offset:1024 lds\n"
    "v_mfma_f32_16x16x32_bf16 a[148:151], a[212:215], v[184:187], a[148:151]\n"
    "buffer_load_dwordx4 v9, s[40:43], s64 offen offset:2048 lds\n"
    "v_mfma_f32_16x16x32_bf16 a[152:155], a[216:219], v[184:187], a[152:155]\n"
    "buffer_load_dwordx4 v10, s[40:43], s64 offen offset:3072 lds\n"
    "v_mfma_f32_16x16x32_bf16 a[156:159], a[220:223], v[184:187], a[156:159]\n"
    "s_waitcnt lgkmcnt(0)\n"
    "s_waitcnt vmcnt(12)\n"
    "s_barrier\n"
    "v_mfma_f32_16x16x32_bf16 a[0:3], a[224:227], v[124:127], a[0:3]\n"
    "v_add_u32 v232, 0x4000, v232\n"
    "v_mfma_f32_16x16x32_bf16 a[4:7], a[228:231], v[124:127], a[4:7]\n"
    "v_add_u32 v233, 0x4000, v233\n"
    "s_add_u32 s64, s63, s59\n"
    "v_mfma_f32_16x16x32_bf16 a[8:11], a[232:235], v[124:127], a[8:11]\n"
    "s_xor_b32 s64, s64, 2\n"
    "v_mfma_f32_16x16x32_bf16 a[12:15], a[236:239], v[124:127], a[12:15]\n"
    "ds_read_b64_tr_b16 a[192:193], v232 offset:0\n"
    "s_lshl_b32 s64, s64, 17\n"
    "v_mfma_f32_16x16x32_bf16 a[16:19], a[240:243], v[124:127], a[16:19]\n"
    "ds_read_b64_tr_b16 a[194:195], v232 offset:4096\n"
    "s_add_u32 s64, s64, s86\n"
    "v_mfma_f32_16x16x32_bf16 a[20:23], a[244:247], v[124:127], a[20:23]\n"
    "v_mfma_f32_16x16x32_bf16 a[24:27], a[248:251], v[124:127], a[24:27]\n"
    "ds_read_b64_tr_b16 a[196:197], v233 offset:0\n"
    "s_add_u32 s64, s64, 0\n"
    "v_mfma_f32_16x16x32_bf16 a[28:31], a[252:255], v[124:127], a[28:31]\n"
    "ds_read_b64_tr_b16 a[198:199], v233 offset:4096\n"
    "s_add_u32 m0, s78, 98304\n"
    "v_mfma_f32_16x16x32_bf16 a[32:35], a[224:227], v[140:143], a[32:35]\n"
    "buffer_load_dwordx4 v7, s[40:43], s64 offen offset:0 lds\n"
    "v_mfma_f32_16x16x32_bf16 a[36:39], a[228:231], v[140:143], a[36:39]\n"
    "ds_read_b64_tr_b16 a[200:201], v232 offset:512\n"
    "v_mfma_f32_16x16x32_bf16 a[40:43], a[232:235], v[140:143], a[40:43]\n"
    "ds_read_b64_tr_b16 a[202:203], v232 offset:4608\n"
    "buffer_load_dwordx4 v8, s[40:43], s64 offen offset:1024 lds\n"
    "v_mfma_f32_16x16x32_bf16 a[44:47], a[236:239], v[140:143], a[44:47]\n"
    "buffer_load_dwordx4 v9, s[40:43], s64 offen offset:2048 lds\n"
    "v_mfma_f32_16x16x32_bf16 a[48:51], a[240:243], v[140:143], a[48:51]\n"
    "ds_read_b64_tr_b16 a[204:205], v233 offset:512\n"
    "buffer_load_dwordx4 v10, s[40:43], s64 offen offset:3072 lds\n"
    "v_mfma_f32_16x16x32_bf16 a[52:55], a[244:247], v[140:143], a[52:55]\n"
    "ds_read_b64_tr_b16 a[206:207], v233 offset:4608\n"
    "v_mfma_f32_16x16x32_bf16 a[56:59], a[248:251], v[140:143], a[56:59]\n"
    "s_add_u32 s64, s63, s59\n"
    "v_mfma_f32_16x16x32_bf16 a[60:63], a[252:255], v[140:143], a[60:63]\n"
    "ds_read_b64_tr_b16 a[208:209], v232 offset:1024\n"
    "s_xor_b32 s64, s64, 2\n"
    "v_mfma_f32_16x16x32_bf16 a[64:67], a[224:227], v[156:159], a[64:67]\n"
    "ds_read_b64_tr_b16 a[210:211], v232 offset:5120\n"
    "s_lshl_b32 s64, s64, 17\n"
    "v_mfma_f32_16x16x32_bf16 a[68:71], a[228:231], v[156:159], a[68:71]\n"
    "s_add_u32 s64, s64, s86\n"
    "v_mfma_f32_16x16x32_bf16 a[72:75], a[232:235], v[156:159], a[72:75]\n"
    "ds_read_b64_tr_b16 a[212:213], v233 offset:1024\n"
    "v_mfma_f32_16x16x32_bf16 a[76:79], a[236:239], v[156:159], a[76:79]\n"
    "ds_read_b64_tr_b16 a[214:215], v233 offset:5120\n"
    "s_add_u32 s64, s64, 32768\n"
    "v_mfma_f32_16x16x32_bf16 a[80:83], a[240:243], v[156:159], a[80:83]\n"
    "s_add_u32 m0, s78, 114688\n"
    "v_mfma_f32_16x16x32_bf16 a[84:87], a[244:247], v[156:159], a[84:87]\n"
    "ds_read_b64_tr_b16 a[216:217], v232 offset:1536\n"
    "buffer_load_dwordx4 v7, s[40:43], s64 offen offset:0 lds\n"
    "v_mfma_f32_16x16x32_bf16 a[88:91], a[248:251], v[156:159], a[88:91]\n"
    "ds_read_b64_tr_b16 a[218:219], v232 offset:5632\n"
    "v_mfma_f32_16x16x32_bf16 a[92:95], a[252:255], v[156:159], a[92:95]\n"
    "buffer_load_dwordx4 v8, s[40:43], s64 offen offset:1024 lds\n"
    "v_mfma_f32_16x16x32_bf16 a[96:99], a[224:227], v[172:175], a[96:99]\n"
    "ds_read_b64_tr_b16 a[220:221], v233 offset:1536\n"
    "buffer_load_dwordx4 v9, s[40:43], s64 offen offset:2048 lds\n"
    "v_mfma_f32_16x16x32_bf16 a[100:103], a[228:231], v[172:175], a[100:103]\n"
    "ds_read_b64_tr_b16 a[222:223], v233 offset:5632\n"
    "buffer_load_dwordx4 v10, s[40:43], s64 offen offset:3072 lds\n"
    "v_mfma_f32_16x16x32_bf16 a[104:107], a[232:235], v[172:175], a[104:107]\n"
    "v_mfma_f32_16x16x32_bf16 a[108:111], a[236:239], v[172:175], a[108:111]\n"
    "s_add_u32 s64, s63, s59\n"
    "v_mfma_f32_16x16x32_bf16 a[112:115], a[240:243], v[172:175], a[112:115]\n"
    "s_xor_b32 s64, s64, 2\n"
    "v_mfma_f32_16x16x32_bf16 a[116:119], a[244:247], v[172:175], a[116:119]\n"
    "s_lshl_b32 s64, s64, 17\n"
    "v_mfma_f32_16x16x32_bf16 a[120:123], a[248:251], v[172:175], a[120:123]\n"
    "s_add_u32 s64, s64, s86\n"
    "v_mfma_f32_16x16x32_bf16 a[124:127], a[252:255], v[172:175], a[124:127]\n"
    "v_mfma_f32_16x16x32_bf16 a[128:131], a[224:227], v[188:191], a[128:131]\n"
    "s_add_u32 s64, s64, 65536\n"
    "v_mfma_f32_16x16x32_bf16 a[132:135], a[228:231], v[188:191], a[132:135]\n"
    "s_add_u32 m0, s78, 131072\n"
    "v_mfma_f32_16x16x32_bf16 a[136:139], a[232:235], v[188:191], a[136:139]\n"
    "buffer_load_dwordx4 v7, s[40:43], s64 offen offset:0 lds\n"
    "v_mfma_f32_16x16x32_bf16 a[140:143], a[236:239], v[188:191], a[140:143]\n"
    "v_mfma_f32_16x16x32_bf16 a[144:147], a[240:243], v[188:191], a[144:147]\n"
    "buffer_load_dwordx4 v8, s[40:43], s64 offen offset:1024 lds\n"
    "v_mfma_f32_16x16x32_bf16 a[148:151], a[244:247], v[188:191], a[148:151]\n"
    "buffer_load_dwordx4 v9, s[40:43], s64 offen offset:2048 lds\n"
    "v_mfma_f32_16x16x32_bf16 a[152:155], a[248:251], v[188:191], a[152:155]\n"
    "buffer_load_dwordx4 v10, s[40:43], s64 offen offset:3072 lds\n"
    "v_mfma_f32_16x16x32_bf16 a[156:159], a[252:255], v[188:191], a[156:159]\n"
    "s_waitcnt lgkmcnt(0)\n"
    "s_waitcnt vmcnt(20)\n"
    "s_barrier\n"
    "s_or_b32 s69, s58, s81\n"
    "s_cmp_lg_u32 s69, 0\n"
    "s_cbranch_scc1 HSF_DONE\n"
    "s_mov_b32 s82, 0\n"
    "HSF_LOOP:\n"
    "s_waitcnt vmcnt(0)\n"
    "s_mov_b64 exec, 0xff\n"
    "v_cmp_eq_u32 vcc, 1, v238\n"
    "s_nop 2\n"
    "v_cndmask_b32 v239, v239, v236, vcc\n"
    "v_cndmask_b32 v251, v238, 2, vcc\n"
    "v_cmp_eq_u32 s[88:89], s80, v237\n"
    "v_cmp_eq_u32 vcc, 0, v251\n"
    "s_nop 1\n"
    "s_nop 2\n"
    "s_and_b64 vcc, vcc, s[88:89]\n"
    "s_nop 1\n"
    "v_cndmask_b32 v238, v251, 1, vcc\n"
    "global_store_dword v242, v236, s[56:57] sc1\n"
    "v_cmp_eq_u32 vcc, 2, v238\n"
    "s_nop 1\n"
    "s_nop 2\n"
    "s_cmp_eq_u64 vcc, 0xff\n"
    "s_cselect_b32 s81, 1, 0\n"
    "global_load_dword v236, v240, s[56:57] sc1\n"
    "global_load_dword v237, v241, s[56:57] sc1\n"
    "s_mov_b64 exec, -1\n"
    "s_add_u32 s82, s82, 1\n"
    "s_cmp_gt_u32 s82, 0x20000\n"
    "s_cbranch_scc1 HSF_END\n"
    "s_sleep 1\n"
    "s_cmp_eq_u32 s81, 0\n"
    "s_cbranch_scc1 HSF_LOOP\n"
    "HSF_END:\n"
    "s_waitcnt vmcnt(0)\n"
    "HSF_DONE:\n"
    "s_cmp_lg_u32 s58, 0\n"
    "s_cbranch_scc1 D1W_SKIP\n"
    "s_mov_b64 exec, 0x1\n"
    "v_add_u32 v250, 131072, v240\n"
    "s_mov_b32 s82, 0\n"
    "D1W_LOOP:\n"
    "global_load_dword v251, v250, s[56:57] sc1\n"
    "s_waitcnt vmcnt(0)\n"
    "v_cmp_ne_u32 vcc, v251, v239\n"
    "s_add_u32 s82, s82, 1\n"
    "s_cmp_gt_u32 s82, 0x20000\n"
    "s_cbranch_scc1 D1W_END\n"
    "s_nop 2\n"
    "s_cbranch_vccz D1W_END\n"
    "s_sleep 1\n"
    "s_branch D1W_LOOP\n"
    "D1W_END:\n"
    "s_mov_b64 exec, -1\n"
    "D1W_SKIP:\n"
    "s_barrier\n"
    "s_waitcnt lgkmcnt(0)\n"
    "v_mfma_f32_16x16x32_bf16 a[0:3], a[192:195], v[32:35], a[0:3]\n"
    "v_add_u32 v232, 0x4000, v232\n"
    "v_mfma_f32_16x16x32_bf16 a[4:7], a[196:199], v[32:35], a[4:7]\n"
    "v_add_u32 v233, 0x4000, v233\n"
    "s_add_u32 s66, s63, s59\n"
    "v_mfma_f32_16x16x32_bf16 a[8:11], a[200:203], v[32:35], a[8:11]\n"
    "s_xor_b32 s66, s66, 2\n"
    "v_mfma_f32_16x16x32_bf16 a[12:15], a[204:207], v[32:35], a[12:15]\n"
    "ds_read_b64_tr_b16 a[224:225], v232 offset:0\n"
    "v_mfma_f32_16x16x32_bf16 a[16:19], a[208:211], v[32:35], a[16:19]\n"
    "ds_read_b64_tr_b16 a[226:227], v232 offset:4096\n"
    "s_mul_i32 s66, s66, 40960\n"
    "v_mfma_f32_16x16x32_bf16 a[20:23], a[212:215], v[32:35], a[20:23]\n"
    "v_mfma_f32_16x16x32_bf16 a[24:27], a[216:219], v[32:35], a[24:27]\n"
    "ds_read_b64_tr_b16 a[228:229], v233 offset:0\n"
    "s_mul_i32 s67, s60, 20480\n"
    "v_mfma_f32_16x16x32_bf16 a[28:31], a[220:223], v[32:35], a[28:31]\n"
    "ds_read_b64_tr_b16 a[230:231], v233 offset:4096\n"
    "s_add_u32 s66, s66, s67\n"
    "v_mfma_f32_16x16x32_bf16 a[32:35], a[192:195], v[48:51], a[32:35]\n"
    "v_mfma_f32_16x16x32_bf16 a[36:39], a[196:199], v[48:51], a[36:39]\n"
    "ds_read_b64_tr_b16 a[232:233], v232 offset:512\n"
    "s_add_u32 s67, s66, 0\n"
    "v_mfma_f32_16x16x32_bf16 a[40:43], a[200:203], v[48:51], a[40:43]\n"
    "ds_read_b64_tr_b16 a[234:235], v232 offset:4608\n"
    "v_mfma_f32_16x16x32_bf16 a[44:47], a[204:207], v[48:51], a[44:47]\n"
    "buffer_load_dwordx4 v[112:115], v234, s[44:47], s67 offen offset:0 sc1\n"
    "v_mfma_f32_16x16x32_bf16 a[48:51], a[208:211], v[48:51], a[48:51]\n"
    "ds_read_b64_tr_b16 a[236:237], v233 offset:512\n"
    "v_mfma_f32_16x16x32_bf16 a[52:55], a[212:215], v[48:51], a[52:55]\n"
    "ds_read_b64_tr_b16 a[238:239], v233 offset:4608\n"
    "s_add_u32 s67, s66, 4096\n"
    "v_mfma_f32_16x16x32_bf16 a[56:59], a[216:219], v[48:51], a[56:59]\n"
    "buffer_load_dwordx4 v[128:131], v234, s[44:47], s67 offen offset:0 sc1\n"
    "v_mfma_f32_16x16x32_bf16 a[60:63], a[220:223], v[48:51], a[60:63]\n"
    "ds_read_b64_tr_b16 a[240:241], v232 offset:1024\n"
    "v_mfma_f32_16x16x32_bf16 a[64:67], a[192:195], v[64:67], a[64:67]\n"
    "ds_read_b64_tr_b16 a[242:243], v232 offset:5120\n"
    "s_add_u32 s67, s66, 8192\n"
    "v_mfma_f32_16x16x32_bf16 a[68:71], a[196:199], v[64:67], a[68:71]\n"
    "v_mfma_f32_16x16x32_bf16 a[72:75], a[200:203], v[64:67], a[72:75]\n"
    "ds_read_b64_tr_b16 a[244:245], v233 offset:1024\n"
    "buffer_load_dwordx4 v[144:147], v234, s[44:47], s67 offen offset:0 sc1\n"
    "v_mfma_f32_16x16x32_bf16 a[76:79], a[204:207], v[64:67], a[76:79]\n"
    "ds_read_b64_tr_b16 a[246:247], v233 offset:5120\n"
    "v_mfma_f32_16x16x32_bf16 a[80:83], a[208:211], v[64:67], a[80:83]\n"
    "s_add_u32 s67, s66, 12288\n"
    "v_mfma_f32_16x16x32_bf16 a[84:87], a[212:215], v[64:67], a[84:87]\n"
    "ds_read_b64_tr_b16 a[248:249], v232 offset:1536\n"
    "buffer_load_dwordx4 v[160:163], v234, s[44:47], s67 offen offset:0 sc1\n"
    "v_mfma_f32_16x16x32_bf16 a[88:91], a[216:219], v[64:67], a[88:91]\n"
    "ds_read_b64_tr_b16 a[250:251], v232 offset:5632\n"
    "v_mfma_f32_16x16x32_bf16 a[92:95], a[220:223], v[64:67], a[92:95]\n"
    "s_add_u32 s67, s66, 16384\n"
    "v_mfma_f32_16x16x32_bf16 a[96:99], a[192:195], v[80:83], a[96:99]\n"
    "ds_read_b64_tr_b16 a[252:253], v233 offset:1536\n"
    "v_mfma_f32_16x16x32_bf16 a[100:103], a[196:199], v[80:83], a[100:103]\n"
    "ds_read_b64_tr_b16 a[254:255], v233 offset:5632\n"
    "buffer_load_dwordx4 v[176:179], v234, s[44:47], s67 offen offset:0 sc1\n"
    "v_mfma_f32_16x16x32_bf16 a[104:107], a[200:203], v[80:83], a[104:107]\n"
    "v_mfma_f32_16x16x32_bf16 a[108:111], a[204:207], v[80:83], a[108:111]\n"
    "s_add_u32 s64, s63, s59\n"
    "v_mfma_f32_16x16x32_bf16 a[112:115], a[208:211], v[80:83], a[112:115]\n"
    "s_xor_b32 s64, s64, 2\n"
    "v_mfma_f32_16x16x32_bf16 a[116:119], a[212:215], v[80:83], a[116:119]\n"
    "s_lshl_b32 s64, s64, 17\n"
    "v_mfma_f32_16x16x32_bf16 a[120:123], a[216:219], v[80:83], a[120:123]\n"
    "s_add_u32 s64, s64, s86\n"
    "v_mfma_f32_16x16x32_bf16 a[124:127], a[220:223], v[80:83], a[124:127]\n"
    "v_mfma_f32_16x16x32_bf16 a[128:131], a[192:195], v[96:99], a[128:131]\n"
    "s_add_u32 s64, s64, 98304\n"
    "v_mfma_f32_16x16x32_bf16 a[132:135], a[196:199], v[96:99], a[132:135]\n"
    "s_add_u32 m0, s78, 147456\n"
    "v_mfma_f32_16x16x32_bf16 a[136:139], a[200:203], v[96:99], a[136:139]\n"
    "buffer_load_dwordx4 v7, s[40:43], s64 offen offset:0 lds\n"
    "v_mfma_f32_16x16x32_bf16 a[140:143], a[204:207], v[96:99], a[140:143]\n"
    "v_mfma_f32_16x16x32_bf16 a[144:147], a[208:211], v[96:99], a[144:147]\n"
    "buffer_load_dwordx4 v8, s[40:43], s64 offen offset:1024 lds\n"
    "v_mfma_f32_16x16x32_bf16 a[148:151], a[212:215], v[96:99], a[148:151]\n"
    "buffer_load_dwordx4 v9, s[40:43], s64 offen offset:2048 lds\n"
    "v_mfma_f32_16x16x32_bf16 a[152:155], a[216:219], v[96:99], a[152:155]\n"
    "buffer_load_dwordx4 v10, s[40:43], s64 offen offset:3072 lds\n"
    "v_mfma_f32_16x16x32_bf16 a[156:159], a[220:223], v[96:99], a[156:159]\n"
    "s_waitcnt lgkmcnt(0)\n"
    "s_waitcnt vmcnt(25)\n"
    "s_barrier\n"
    "v_mfma_f32_16x16x32_bf16 a[0:3], a[224:227], v[36:39], a[0:3]\n"
    "v_add_u32 v232, 0x4000, v232\n"
    "v_mfma_f32_16x16x32_bf16 a[4:7], a[228:231], v[36:39], a[4:7]\n"
    "v_add_u32 v233, 0x4000, v233\n"
    "s_add_u32 s67, s66, 0\n"
    "v_mfma_f32_16x16x32_bf16 a[8:11], a[232:235], v[36:39], a[8:11]\n"
    "v_mfma_f32_16x16x32_bf16 a[12:15], a[236:239], v[36:39], a[12:15]\n"
    "ds_read_b64_tr_b16 a[192:193], v232 offset:0\n"
    "buffer_load_dwordx4 v[116:119], v234, s[44:47], s67 offen offset:1024 sc1\n"
    "v_mfma_f32_16x16x32_bf16 a[16:19], a[240:243], v[36:39], a[16:19]\n"
    "ds_read_b64_tr_b16 a[194:195], v232 offset:4096\n"
    "v_mfma_f32_16x16x32_bf16 a[20:23], a[244:247], v[36:39], a[20:23]\n"
    "v_mfma_f32_16x16x32_bf16 a[24:27], a[248:251], v[36:39], a[24:27]\n"
    "ds_read_b64_tr_b16 a[196:197], v233 offset:0\n"
    "s_add_u32 s67, s66, 4096\n"
    "v_mfma_f32_16x16x32_bf16 a[28:31], a[252:255], v[36:39], a[28:31]\n"
    "ds_read_b64_tr_b16 a[198:199], v233 offset:4096\n"
    "v_mfma_f32_16x16x32_bf16 a[32:35], a[224:227], v[52:55], a[32:35]\n"
    "buffer_load_dwordx4 v[132:135], v234, s[44:47], s67 offen offset:1024 sc1\n"
    "v_mfma_f32_16x16x32_bf16 a[36:39], a[228:231], v[52:55], a[36:39]\n"
    "ds_read_b64_tr_b16 a[200:201], v232 offset:512\n"
    "v_mfma_f32_16x16x32_bf16 a[40:43], a[232:235], v[52:55], a[40:43]\n"
    "ds_read_b64_tr_b16 a[202:203], v232 offset:4608\n"
    "v_mfma_f32_16x16x32_bf16 a[44:47], a[236:239], v[52:55], a[44:47]\n"
    "s_add_u32 s67, s66, 8192\n"
    "v_mfma_f32_16x16x32_bf16 a[48:51], a[240:243], v[52:55], a[48:51]\n"
    "ds_read_b64_tr_b16 a[204:205], v233 offset:512\n"
    "v_mfma_f32_16x16x32_bf16 a[52:55], a[244:247], v[52:55], a[52:55]\n"
    "ds_read_b64_tr_b16 a[206:207], v233 offset:4608\n"
    "v_mfma_f32_16x16x32_bf16 a[56:59], a[248:251], v[52:55], a[56:59]\n"
    "buffer_load_dwordx4 v[148:151], v234, s[44:47], s67 offen offset:1024 sc1\n"
    "v_mfma_f32_16x16x32_bf16 a[60:63], a[252:255], v[52:55], a[60:63]\n"
    "ds_read_b64_tr_b16 a[208:209], v232 offset:1024\n"
    "v_mfma_f32_16x16x32_bf16 a[64:67], a[224:227], v[68:71], a[64:67]\n"
    "ds_read_b64_tr_b16 a[210:211], v232 offset:5120\n"
    "s_add_u32 s67, s66, 12288\n"
    "v_mfma_f32_16x16x32_bf16 a[68:71], a[228:231], v[68:71], a[68:71]\n"
    "v_mfma_f32_16x16x32_bf16 a[72:75], a[232:235], v[68:71], a[72:75]\n"
    "ds_read_b64_tr_b16 a[212:213], v233 offset:1024\n"
    "v_mfma_f32_16x16x32_bf16 a[76:79], a[236:239], v[68:71], a[76:79]\n"
    "ds_read_b64_tr_b16 a[214:215], v233 offset:5120\n"
    "buffer_load_dwordx4 v[164:167], v234, s[44:47], s67 offen offset:1024 sc1\n"
    "v_mfma_f32_16x16x32_bf16 a[80:83], a[240:243], v[68:71], a[80:83]\n"
    "v_mfma_f32_16x16x32_bf16 a[84:87], a[244:247], v[68:71], a[84:87]\n"
    "ds_read_b64_tr_b16 a[216:217], v232 offset:1536\n"
    "s_add_u32 s67, s66, 16384\n"
    "v_mfma_f32_16x16x32_bf16 a[88:91], a[248:251], v[68:71], a[88:91]\n"
    "ds_read_b64_tr_b16 a[218:219], v232 offset:5632\n"
    "v_mfma_f32_16x16x32_bf16 a[92:95], a[252:255], v[68:71], a[92:95]\n"
    "v_mfma_f32_16x16x32_bf16 a[96:99], a[224:227], v[84:87], a[96:99]\n"
    "ds_read_b64_tr_b16 a[220:221], v233 offset:1536\n"
    "buffer_load_dwordx4 v[180:183], v234, s[44:47], s67 offen offset:1024 sc1\n"
    "v_mfma_f32_16x16x32_bf16 a[100:103], a[228:231], v[84:87], a[100:103]\n"
    "ds_read_b64_tr_b16 a[222:223], v233 offset:5632\n"
    "v_mfma_f32_16x16x32_bf16 a[104:107], a[232:235], v[84:87], a[104:107]\n"
    "v_mfma_f32_16x16x32_bf16 a[108:111], a[236:239], v[84:87], a[108:111]\n"
    "s_add_u32 s64, s63, s59\n"
    "v_mfma_f32_16x16x32_bf16 a[112:115], a[240:243], v[84:87], a[112:115]\n"
    "s_xor_b32 s64, s64, 3\n"
    "v_mfma_f32_16x16x32_bf16 a[116:119], a[244:247], v[84:87], a[116:119]\n"
    "s_lshl_b32 s64, s64, 17\n"
    "v_mfma_f32_16x16x32_bf16 a[120:123], a[248:251], v[84:87], a[120:123]\n"
    "s_add_u32 s64, s64, s86\n"
    "v_mfma_f32_16x16x32_bf16 a[124:127], a[252:255], v[84:87], a[124:127]\n"
    "v_mfma_f32_16x16x32_bf16 a[128:131], a[224:227], v[100:103], a[128:131]\n"
    "s_add_u32 s64, s64, 0\n"
    "v_mfma_f32_16x16x32_bf16 a[132:135], a[228:231], v[100:103], a[132:135]\n"
    "s_add_u32 m0, s78, 0\n"
    "v_mfma_f32_16x16x32_bf16 a[136:139], a[232:235], v[100:103], a[136:139]\n"
    "buffer_load_dwordx4 v7, s[40:43], s64 offen offset:0 lds\n"
    "v_mfma_f32_16x16x32_bf16 a[140:143], a[236:239], v[100:103], a[140:143]\n"
    "v_mfma_f32_16x16x32_bf16 a[144:147], a[240:243], v[100:103], a[144:147]\n"
    "buffer_load_dwordx4 v8, s[40:43], s64 offen offset:1024 lds\n"
    "v_mfma_f32_16x16x32_bf16 a[148:151], a[244:247], v[100:103], a[148:151]\n"
    "buffer_load_dwordx4 v9, s[40:43], s64 offen offset:2048 lds\n"
    "v_mfma_f32_16x16x32_bf16 a[152:155], a[248:251], v[100:103], a[152:155]\n"
    "buffer_load_dwordx4 v10, s[40:43], s64 offen offset:3072 lds\n"
    "v_mfma_f32_16x16x32_bf16 a[156:159], a[252:255], v[100:103], a[156:159]\n"
    "s_waitcnt lgkmcnt(0)\n"
    "s_waitcnt vmcnt(30)\n"
    "s_barrier\n"
    "v_mfma_f32_16x16x32_bf16 a[0:3], a[192:195], v[40:43], a[0:3]\n"
    "v_add_u32 v232, 0x4000, v232\n"
    "v_mfma_f32_16x16x32_bf16 a[4:7], a[196:199], v[40:43], a[4:7]\n"
    "v_add_u32 v233, 0x4000, v233\n"
    "s_add_u32 s67, s66, 0\n"
    "v_mfma_f32_16x16x32_bf16 a[8:11], a[200:203], v[40:43], a[8:11]\n"
    "v_mfma_f32_16x16x32_bf16 a[12:15], a[204:207], v[40:43], a[12:15]\n"
    "ds_read_b64_tr_b16 a[224:225], v232 offset:0\n"
    "buffer_load_dwordx4 v[120:123], v234, s[44:47], s67 offen offset:2048 sc1\n"
    "v_mfma_f32_16x16x32_bf16 a[16:19], a[208:211], v[40:43], a[16:19]\n"
    "ds_read_b64_tr_b16 a[226:227], v232 offset:4096\n"
    "v_mfma_f32_16x16x32_bf16 a[20:23], a[212:215], v[40:43], a[20:23]\n"
    "v_mfma_f32_16x16x32_bf16 a[24:27], a[216:219], v[40:43], a[24:27]\n"
    "ds_read_b64_tr_b16 a[228:229], v233 offset:0\n"
    "s_add_u32 s67, s66, 4096\n"
    "v_mfma_f32_16x16x32_bf16 a[28:31], a[220:223], v[40:43], a[28:31]\n"
    "ds_read_b64_tr_b16 a[230:231], v233 offset:4096\n"
    "v_mfma_f32_16x16x32_bf16 a[32:35], a[192:195], v[56:59], a[32:35]\n"
    "buffer_load_dwordx4 v[136:139], v234, s[44:47], s67 offen offset:2048 sc1\n"
    "v_mfma_f32_16x16x32_bf16 a[36:39], a[196:199], v[56:59], a[36:39]\n"
    "ds_read_b64_tr_b16 a[232:233], v232 offset:512\n"
    "v_mfma_f32_16x16x32_bf16 a[40:43], a[200:203], v[56:59], a[40:43]\n"
    "ds_read_b64_tr_b16 a[234:235], v232 offset:4608\n"
    "v_mfma_f32_16x16x32_bf16 a[44:47], a[204:207], v[56:59], a[44:47]\n"
    "s_add_u32 s67, s66, 8192\n"
    "v_mfma_f32_16x16x32_bf16 a[48:51], a[208:211], v[56:59], a[48:51]\n"
    "ds_read_b64_tr_b16 a[236:237], v233 offset:512\n"
    "v_mfma_f32_16x16x32_bf16 a[52:55], a[212:215], v[56:59], a[52:55]\n"
    "ds_read_b64_tr_b16 a[238:239], v233 offset:4608\n"
    "v_mfma_f32_16x16x32_bf16 a[56:59], a[216:219], v[56:59], a[56:59]\n"
    "buffer_load_dwordx4 v[152:155], v234, s[44:47], s67 offen offset:2048 sc1\n"
    "v_mfma_f32_16x16x32_bf16 a[60:63], a[220:223], v[56:59], a[60:63]\n"
    "ds_read_b64_tr_b16 a[240:241], v232 offset:1024\n"
    "v_mfma_f32_16x16x32_bf16 a[64:67], a[192:195], v[72:75], a[64:67]\n"
    "ds_read_b64_tr_b16 a[242:243], v232 offset:5120\n"
    "s_add_u32 s67, s66, 12288\n"
    "v_mfma_f32_16x16x32_bf16 a[68:71], a[196:199], v[72:75], a[68:71]\n"
    "v_mfma_f32_16x16x32_bf16 a[72:75], a[200:203], v[72:75], a[72:75]\n"
    "ds_read_b64_tr_b16 a[244:245], v233 offset:1024\n"
    "v_mfma_f32_16x16x32_bf16 a[76:79], a[204:207], v[72:75], a[76:79]\n"
    "ds_read_b64_tr_b16 a[246:247], v233 offset:5120\n"
    "buffer_load_dwordx4 v[168:171], v234, s[44:47], s67 offen offset:2048 sc1\n"
    "v_mfma_f32_16x16x32_bf16 a[80:83], a[208:211], v[72:75], a[80:83]\n"
    "v_mfma_f32_16x16x32_bf16 a[84:87], a[212:215], v[72:75], a[84:87]\n"
    "ds_read_b64_tr_b16 a[248:249], v232 offset:1536\n"
    "s_add_u32 s67, s66, 16384\n"
    "v_mfma_f32_16x16x32_bf16 a[88:91], a[216:219], v[72:75], a[88:91]\n"
    "ds_read_b64_tr_b16 a[250:251], v232 offset:5632\n"
    "v_mfma_f32_16x16x32_bf16 a[92:95], a[220:223], v[72:75], a[92:95]\n"
    "v_mfma_f32_16x16x32_bf16 a[96:99], a[192:195], v[88:91], a[96:99]\n"
    "ds_read_b64_tr_b16 a[252:253], v233 offset:1536\n"
    "buffer_load_dwordx4 v[184:187], v234, s[44:47], s67 offen offset:2048 sc1\n"
    "v_mfma_f32_16x16x32_bf16 a[100:103], a[196:199], v[88:91], a[100:103]\n"
    "ds_read_b64_tr_b16 a[254:255], v233 offset:5632\n"
    "v_mfma_f32_16x16x32_bf16 a[104:107], a[200:203], v[88:91], a[104:107]\n"
    "v_mfma_f32_16x16x32_bf16 a[108:111], a[204:207], v[88:91], a[108:111]\n"
    "s_add_u32 s64, s63, s59\n"
    "v_mfma_f32_16x16x32_bf16 a[112:115], a[208:211], v[88:91], a[112:115]\n"
    "s_xor_b32 s64, s64, 3\n"
    "v_mfma_f32_16x16x32_bf16 a[116:119], a[212:215], v[88:91], a[116:119]\n"
    "s_lshl_b32 s64, s64, 17\n"
    "v_mfma_f32_16x16x32_bf16 a[120:123], a[216:219], v[88:91], a[120:123]\n"
    "s_add_u32 s64, s64, s86\n"
    "v_mfma_f32_16x16x32_bf16 a[124:127], a[220:223], v[88:91], a[124:127]\n"
    "v_mfma_f32_16x16x32_bf16 a[128:131], a[192:195], v[104:107], a[128:131]\n"
    "s_add_u32 s64, s64, 32768\n"
    "v_mfma_f32_16x16x32_bf16 a[132:135], a[196:199], v[104:107], a[132:135]\n"
    "s_add_u32 m0, s78, 16384\n"
    "v_mfma_f32_16x16x32_bf16 a[136:139], a[200:203], v[104:107], a[136:139]\n"
    "buffer_load_dwordx4 v7, s[40:43], s64 offen offset:0 lds\n"
    "v_mfma_f32_16x16x32_bf16 a[140:143], a[204:207], v[104:107], a[140:143]\n"
    "v_mfma_f32_16x16x32_bf16 a[144:147], a[208:211], v[104:107], a[144:147]\n"
    "buffer_load_dwordx4 v8, s[40:43], s64 offen offset:1024 lds\n"
    "v_mfma_f32_16x16x32_bf16 a[148:151], a[212:215], v[104:107], a[148:151]\n"
    "buffer_load_dwordx4 v9, s[40:43], s64 offen offset:2048 lds\n"
    "v_mfma_f32_16x16x32_bf16 a[152:155], a[216:219], v[104:107], a[152:155]\n"
    "buffer_load_dwordx4 v10, s[40:43], s64 offen offset:3072 lds\n"
    "v_mfma_f32_16x16x32_bf16 a[156:159], a[220:223], v[104:107], a[156:159]\n"
    "s_waitcnt lgkmcnt(0)\n"
    "s_waitcnt vmcnt(35)\n"
    "s_barrier\n"
    "v_mfma_f32_16x16x32_bf16 a[0:3], a[224:227], v[44:47], a[0:3]\n"
    "v_add_u32 v232, 0x4000, v232\n"
    "v_mfma_f32_16x16x32_bf16 a[4:7], a[228:231], v[44:47], a[4:7]\n"
    "v_add_u32 v233, 0x4000, v233\n"
    "s_add_u32 s67, s66, 0\n"
    "v_mfma_f32_16x16x32_bf16 a[8:11], a[232:235], v[44:47], a[8:11]\n"
    "v_mfma_f32_16x16x32_bf16 a[12:15], a[236:239], v[44:47], a[12:15]\n"
    "ds_read_b64_tr_b16 a[192:193], v232 offset:0\n"
    "buffer_load_dwordx4 v[124:127], v234, s[44:47], s67 offen offset:3072 sc1\n"
    "v_mfma_f32_16x16x32_bf16 a[16:19], a[240:243], v[44:47], a[16:19]\n"
    "ds_read_b64_tr_b16 a[194:195], v232 offset:4096\n"
    "v_mfma_f32_16x16x32_bf16 a[20:23], a[244:247], v[44:47], a[20:23]\n"
    "v_mfma_f32_16x16x32_bf16 a[24:27], a[248:251], v[44:47], a[24:27]\n"
    "ds_read_b64_tr_b16 a[196:197], v233 offset:0\n"
    "s_add_u32 s67, s66, 4096\n"
    "v_mfma_f32_16x16x32_bf16 a[28:31], a[252:255], v[44:47], a[28:31]\n"
    "ds_read_b64_tr_b16 a[198:199], v233 offset:4096\n"
    "v_mfma_f32_16x16x32_bf16 a[32:35], a[224:227], v[60:63], a[32:35]\n"
    "buffer_load_dwordx4 v[140:143], v234, s[44:47], s67 offen offset:3072 sc1\n"
    "v_mfma_f32_16x16x32_bf16 a[36:39], a[228:231], v[60:63], a[36:39]\n"
    "ds_read_b64_tr_b16 a[200:201], v232 offset:512\n"
    "v_mfma_f32_16x16x32_bf16 a[40:43], a[232:235], v[60:63], a[40:43]\n"
    "ds_read_b64_tr_b16 a[202:203], v232 offset:4608\n"
    "v_mfma_f32_16x16x32_bf16 a[44:47], a[236:239], v[60:63], a[44:47]\n"
    "s_add_u32 s67, s66, 8192\n"
    "v_mfma_f32_16x16x32_bf16 a[48:51], a[240:243], v[60:63], a[48:51]\n"
    "ds_read_b64_tr_b16 a[204:205], v233 offset:512\n"
    "v_mfma_f32_16x16x32_bf16 a[52:55], a[244:247], v[60:63], a[52:55]\n"
    "ds_read_b64_tr_b16 a[206:207], v233 offset:4608\n"
    "v_mfma_f32_16x16x32_bf16 a[56:59], a[248:251], v[60:63], a[56:59]\n"
    "buffer_load_dwordx4 v[156:159], v234, s[44:47], s67 offen offset:3072 sc1\n"
    "v_mfma_f32_16x16x32_bf16 a[60:63], a[252:255], v[60:63], a[60:63]\n"
    "ds_read_b64_tr_b16 a[208:209], v232 offset:1024\n"
    "v_mfma_f32_16x16x32_bf16 a[64:67], a[224:227], v[76:79], a[64:67]\n"
    "ds_read_b64_tr_b16 a[210:211], v232 offset:5120\n"
    "s_add_u32 s67, s66, 12288\n"
    "v_mfma_f32_16x16x32_bf16 a[68:71], a[228:231], v[76:79], a[68:71]\n"
    "v_mfma_f32_16x16x32_bf16 a[72:75], a[232:235], v[76:79], a[72:75]\n"
    "ds_read_b64_tr_b16 a[212:213], v233 offset:1024\n"
    "v_mfma_f32_16x16x32_bf16 a[76:79], a[236:239], v[76:79], a[76:79]\n"
    "ds_read_b64_tr_b16 a[214:215], v233 offset:5120\n"
    "buffer_load_dwordx4 v[172:175], v234, s[44:47], s67 offen offset:3072 sc1\n"
    "v_mfma_f32_16x16x32_bf16 a[80:83], a[240:243], v[76:79], a[80:83]\n"
    "v_mfma_f32_16x16x32_bf16 a[84:87], a[244:247], v[76:79], a[84:87]\n"
    "ds_read_b64_tr_b16 a[216:217], v232 offset:1536\n"
    "s_add_u32 s67, s66, 16384\n"
    "v_mfma_f32_16x16x32_bf16 a[88:91], a[248:251], v[76:79], a[88:91]\n"
    "ds_read_b64_tr_b16 a[218:219], v232 offset:5632\n"
    "v_mfma_f32_16x16x32_bf16 a[92:95], a[252:255], v[76:79], a[92:95]\n"
    "v_mfma_f32_16x16x32_bf16 a[96:99], a[224:227], v[92:95], a[96:99]\n"
    "ds_read_b64_tr_b16 a[220:221], v233 offset:1536\n"
    "buffer_load_dwordx4 v[188:191], v234, s[44:47], s67 offen offset:3072 sc1\n"
    "v_mfma_f32_16x16x32_bf16 a[100:103], a[228:231], v[92:95], a[100:103]\n"
    "ds_read_b64_tr_b16 a[222:223], v233 offset:5632\n"
    "v_mfma_f32_16x16x32_bf16 a[104:107], a[232:235], v[92:95], a[104:107]\n"
    "v_mfma_f32_16x16x32_bf16 a[108:111], a[236:239], v[92:95], a[108:111]\n"
    "s_add_u32 s64, s63, s59\n"
    "v_mfma_f32_16x16x32_bf16 a[112:115], a[240:243], v[92:95], a[112:115]\n"
    "s_xor_b32 s64, s64, 3\n"
    "v_mfma_f32_16x16x32_bf16 a[116:119], a[244:247], v[92:95], a[116:119]\n"
    "s_lshl_b32 s64, s64, 17\n"
    "v_mfma_f32_16x16x32_bf16 a[120:123], a[248:251], v[92:95], a[120:123]\n"
    "s_add_u32 s64, s64, s86\n"
    "v_mfma_f32_16x16x32_bf16 a[124:127], a[252:255], v[92:95], a[124:127]\n"
    "v_mfma_f32_16x16x32_bf16 a[128:131], a[224:227], v[108:111], a[128:131]\n"
    "s_add_u32 s64, s64, 65536\n"
    "v_mfma_f32_16x16x32_bf16 a[132:135], a[228:231], v[108:111], a[132:135]\n"
    "s_add_u32 m0, s78, 32768\n"
    "v_mfma_f32_16x16x32_bf16 a[136:139], a[232:235], v[108:111], a[136:139]\n"
    "buffer_load_dwordx4 v7, s[40:43], s64 offen offset:0 lds\n"
    "v_mfma_f32_16x16x32_bf16 a[140:143], a[236:239], v[108:111], a[140:143]\n"
    "v_mfma_f32_16x16x32_bf16 a[144:147], a[240:243], v[108:111], a[144:147]\n"
    "buffer_load_dwordx4 v8, s[40:43], s64 offen offset:1024 lds\n"
    "v_mfma_f32_16x16x32_bf16 a[148:151], a[244:247], v[108:111], a[148:151]\n"
    "buffer_load_dwordx4 v9, s[40:43], s64 offen offset:2048 lds\n"
    "v_mfma_f32_16x16x32_bf16 a[152:155], a[248:251], v[108:111], a[152:155]\n"
    "buffer_load_dwordx4 v10, s[40:43], s64 offen offset:3072 lds\n"
    "v_mfma_f32_16x16x32_bf16 a[156:159], a[252:255], v[108:111], a[156:159]\n"
    "s_waitcnt lgkmcnt(0)\n"
    "s_waitcnt vmcnt(40)\n"
    "s_barrier\n"
    "s_waitcnt vmcnt(31)\n"
    "v_mfma_f32_16x16x32_bf16 a[0:3], a[192:195], v[112:115], a[0:3]\n"
    "v_add_u32 v232, 0x4000, v232\n"
    "v_mfma_f32_16x16x32_bf16 a[4:7], a[196:199], v[112:115], a[4:7]\n"
    "v_add_u32 v233, 0x4000, v233\n"
    "s_add_u32 s66, s63, s59\n"
    "v_mfma_f32_16x16x32_bf16 a[8:11], a[200:203], v[112:115], a[8:11]\n"
    "s_xor_b32 s66, s66, 3\n"
    "v_mfma_f32_16x16x32_bf16 a[12:15], a[204:207], v[112:115], a[12:15]\n"
    "ds_read_b64_tr_b16 a[224:225], v232 offset:0\n"
    "v_mfma_f32_16x16x32_bf16 a[16:19], a[208:211], v[112:115], a[16:19]\n"
    "ds_read_b64_tr_b16 a[226:227], v232 offset:4096\n"
    "s_mul_i32 s66, s66, 40960\n"
    "v_mfma_f32_16x16x32_bf16 a[20:23], a[212:215], v[112:115], a[20:23]\n"
    "v_mfma_f32_16x16x32_bf16 a[24:27], a[216:219], v[112:115], a[24:27]\n"
    "ds_read_b64_tr_b16 a[228:229], v233 offset:0\n"
    "s_mul_i32 s67, s60, 20480\n"
    "v_mfma_f32_16x16x32_bf16 a[28:31], a[220:223], v[112:115], a[28:31]\n"
    "ds_read_b64_tr_b16 a[230:231], v233 offset:4096\n"
    "s_add_u32 s66, s66, s67\n"
    "v_mfma_f32_16x16x32_bf16 a[32:35], a[192:195], v[128:131], a[32:35]\n"
    "v_mfma_f32_16x16x32_bf16 a[36:39], a[196:199], v[128:131], a[36:39]\n"
    "ds_read_b64_tr_b16 a[232:233], v232 offset:512\n"
    "s_add_u32 s67, s66, 0\n"
    "v_mfma_f32_16x16x32_bf16 a[40:43], a[200:203], v[128:131], a[40:43]\n"
    "ds_read_b64_tr_b16 a[234:235], v232 offset:4608\n"
    "v_mfma_f32_16x16x32_bf16 a[44:47], a[204:207], v[128:131], a[44:47]\n"
    "buffer_load_dwordx4 v[32:35], v234, s[44:47], s67 offen offset:0 sc1\n"
    "v_mfma_f32_16x16x32_bf16 a[48:51], a[208:211], v[128:131], a[48:51]\n"
    "ds_read_b64_tr_b16 a[236:237], v233 offset:512\n"
    "v_mfma_f32_16x16x32_bf16 a[52:55], a[212:215], v[128:131], a[52:55]\n"
    "ds_read_b64_tr_b16 a[238:239], v233 offset:4608\n"
    "s_add_u32 s67, s66, 4096\n"
    "v_mfma_f32_16x16x32_bf16 a[56:59], a[216:219], v[128:131], a[56:59]\n"
    "buffer_load_dwordx4 v[48:51], v234, s[44:47], s67 offen offset:0 sc1\n"
    "v_mfma_f32_16x16x32_bf16 a[60:63], a[220:223], v[128:131], a[60:63]\n"
    "ds_read_b64_tr_b16 a[240:241], v232 offset:1024\n"
    "v_mfma_f32_16x16x32_bf16 a[64:67], a[192:195], v[144:147], a[64:67]\n"
    "ds_read_b64_tr_b16 a[242:243], v232 offset:5120\n"
    "s_add_u32 s67, s66, 8192\n"
    "v_mfma_f32_16x16x32_bf16 a[68:71], a[196:199], v[144:147], a[68:71]\n"
    "v_mfma_f32_16x16x32_bf16 a[72:75], a[200:203], v[144:147], a[72:75]\n"
    "ds_read_b64_tr_b16 a[244:245], v233 offset:1024\n"
    "buffer_load_dwordx4 v[64:67], v234, s[44:47], s67 offen offset:0 sc1\n"
    "v_mfma_f32_16x16x32_bf16 a[76:79], a[204:207], v[144:147], a[76:79]\n"
    "ds_read_b64_tr_b16 a[246:247], v233 offset:5120\n"
    "v_mfma_f32_16x16x32_bf16 a[80:83], a[208:211], v[144:147], a[80:83]\n"
    "s_add_u32 s67, s66, 12288\n"
    "v_mfma_f32_16x16x32_bf16 a[84:87], a[212:215], v[144:147], a[84:87]\n"
    "ds_read_b64_tr_b16 a[248:249], v232 offset:1536\n"
    "buffer_load_dwordx4 v[80:83], v234, s[44:47], s67 offen offset:0 sc1\n"
    "v_mfma_f32_16x16x32_bf16 a[88:91], a[216:219], v[144:147], a[88:91]\n"
    "ds_read_b64_tr_b16 a[250:251], v232 offset:5632\n"
    "v_mfma_f32_16x16x32_bf16 a[92:95], a[220:223], v[144:147], a[92:95]\n"
    "s_add_u32 s67, s66, 16384\n"
    "v_mfma_f32_16x16x32_bf16 a[96:99], a[192:195], v[160:163], a[96:99]\n"
    "ds_read_b64_tr_b16 a[252:253], v233 offset:1536\n"
    "v_mfma_f32_16x16x32_bf16 a[100:103], a[196:199], v[160:163], a[100:103]\n"
    "ds_read_b64_tr_b16 a[254:255], v233 offset:5632\n"
    "buffer_load_dwordx4 v[96:99], v234, s[44:47], s67 offen offset:0 sc1\n"
    "v_mfma_f32_16x16x32_bf16 a[104:107], a[200:203], v[160:163], a[104:107]\n"
    "v_mfma_f32_16x16x32_bf16 a[108:111], a[204:207], v[160:163], a[108:111]\n"
    "s_add_u32 s64, s63, s59\n"
    "v_mfma_f32_16x16x32_bf16 a[112:115], a[208:211], v[160:163], a[112:115]\n"
    "s_xor_b32 s64, s64, 3\n"
    "v_mfma_f32_16x16x32_bf16 a[116:119], a[212:215], v[160:163], a[116:119]\n"
    "s_lshl_b32 s64, s64, 17\n"
    "v_mfma_f32_16x16x32_bf16 a[120:123], a[216:219], v[160:163], a[120:123]\n"
    "s_add_u32 s64, s64, s86\n"
    "v_mfma_f32_16x16x32_bf16 a[124:127], a[220:223], v[160:163], a[124:127]\n"
    "v_mfma_f32_16x16x32_bf16 a[128:131], a[192:195], v[176:179], a[128:131]\n"
    "s_add_u32 s64, s64, 98304\n"
    "v_mfma_f32_16x16x32_bf16 a[132:135], a[196:199], v[176:179], a[132:135]\n"
    "s_add_u32 m0, s78, 49152\n"
    "v_mfma_f32_16x16x32_bf16 a[136:139], a[200:203], v[176:179], a[136:139]\n"
    "buffer_load_dwordx4 v7, s[40:43], s64 offen offset:0 lds\n"
    "v_mfma_f32_16x16x32_bf16 a[140:143], a[204:207], v[176:179], a[140:143]\n"
    "v_mfma_f32_16x16x32_bf16 a[144:147], a[208:211], v[176:179], a[144:147]\n"
    "buffer_load_dwordx4 v8, s[40:43], s64 offen offset:1024 lds\n"
    "v_mfma_f32_16x16x32_bf16 a[148:151], a[212:215], v[176:179], a[148:151]\n"
    "buffer_load_dwordx4 v9, s[40:43], s64 offen offset:2048 lds\n"
    "v_mfma_f32_16x16x32_bf16 a[152:155], a[216:219], v[176:179], a[152:155]\n"
    "buffer_load_dwordx4 v10, s[40:43], s64 offen offset:3072 lds\n"
    "v_mfma_f32_16x16x32_bf16 a[156:159], a[220:223], v[176:179], a[156:159]\n"
    "s_waitcnt lgkmcnt(0)\n"
    "s_waitcnt vmcnt(40)\n"
    "s_barrier\n"
    "s_waitcnt vmcnt(31)\n"
    "v_mfma_f32_16x16x32_bf16 a[0:3], a[224:227], v[116:119], a[0:3]\n"
    "v_add_u32 v232, 0x4000, v232\n"
    "v_mfma_f32_16x16x32_bf16 a[4:7], a[228:231], v[116:119], a[4:7]\n"
    "v_add_u32 v233, 0x4000, v233\n"
    "s_add_u32 s67, s66, 0\n"
    "v_mfma_f32_16x16x32_bf16 a[8:11], a[232:235], v[116:119], a[8:11]\n"
    "v_mfma_f32_16x16x32_bf16 a[12:15], a[236:239], v[116:119], a[12:15]\n"
    "ds_read_b64_tr_b16 a[192:193], v232 offset:0\n"
    "buffer_load_dwordx4 v[36:39], v234, s[44:47], s67 offen offset:1024 sc1\n"
    "v_mfma_f32_16x16x32_bf16 a[16:19], a[240:243], v[116:119], a[16:19]\n"
    "ds_read_b64_tr_b16 a[194:195], v232 offset:4096\n"
    "v_mfma_f32_16x16x32_bf16 a[20:23], a[244:247], v[116:119], a[20:23]\n"
    "v_mfma_f32_16x16x32_bf16 a[24:27], a[248:251], v[116:119], a[24:27]\n"
    "ds_read_b64_tr_b16 a[196:197], v233 offset:0\n"
    "s_add_u32 s67, s66, 4096\n"
    "v_mfma_f32_16x16x32_bf16 a[28:31], a[252:255], v[116:119], a[28:31]\n"
    "ds_read_b64_tr_b16 a[198:199], v233 offset:4096\n"
    "v_mfma_f32_16x16x32_bf16 a[32:35], a[224:227], v[132:135], a[32:35]\n"
    "buffer_load_dwordx4 v[52:55], v234, s[44:47], s67 offen offset:1024 sc1\n"
    "v_mfma_f32_16x16x32_bf16 a[36:39], a[228:231], v[132:135], a[36:39]\n"
    "ds_read_b64_tr_b16 a[200:201], v232 offset:512\n"
    "v_mfma_f32_16x16x32_bf16 a[40:43], a[232:235], v[132:135], a[40:43]\n"
    "ds_read_b64_tr_b16 a[202:203], v232 offset:4608\n"
    "v_mfma_f32_16x16x32_bf16 a[44:47], a[236:239], v[132:135], a[44:47]\n"
    "s_add_u32 s67, s66, 8192\n"
    "v_mfma_f32_16x16x32_bf16 a[48:51], a[240:243], v[132:135], a[48:51]\n"
    "ds_read_b64_tr_b16 a[204:205], v233 offset:512\n"
    "v_mfma_f32_16x16x32_bf16 a[52:55], a[244:247], v[132:135], a[52:55]\n"
    "ds_read_b64_tr_b16 a[206:207], v233 offset:4608\n"
    "v_mfma_f32_16x16x32_bf16 a[56:59], a[248:251], v[132:135], a[56:59]\n"
    "buffer_load_dwordx4 v[68:71], v234, s[44:47], s67 offen offset:1024 sc1\n"
    "v_mfma_f32_16x16x32_bf16 a[60:63], a[252:255], v[132:135], a[60:63]\n"
    "ds_read_b64_tr_b16 a[208:209], v232 offset:1024\n"
    "v_mfma_f32_16x16x32_bf16 a[64:67], a[224:227], v[148:151], a[64:67]\n"
    "ds_read_b64_tr_b16 a[210:211], v232 offset:5120\n"
    "s_add_u32 s67, s66, 12288\n"
    "v_mfma_f32_16x16x32_bf16 a[68:71], a[228:231], v[148:151], a[68:71]\n"
    "v_mfma_f32_16x16x32_bf16 a[72:75], a[232:235], v[148:151], a[72:75]\n"
    "ds_read_b64_tr_b16 a[212:213], v233 offset:1024\n"
    "v_mfma_f32_16x16x32_bf16 a[76:79], a[236:239], v[148:151], a[76:79]\n"
    "ds_read_b64_tr_b16 a[214:215], v233 offset:5120\n"
    "buffer_load_dwordx4 v[84:87], v234, s[44:47], s67 offen offset:1024 sc1\n"
    "v_mfma_f32_16x16x32_bf16 a[80:83], a[240:243], v[148:151], a[80:83]\n"
    "v_mfma_f32_16x16x32_bf16 a[84:87], a[244:247], v[148:151], a[84:87]\n"
    "ds_read_b64_tr_b16 a[216:217], v232 offset:1536\n"
    "s_add_u32 s67, s66, 16384\n"
    "v_mfma_f32_16x16x32_bf16 a[88:91], a[248:251], v[148:151], a[88:91]\n"
    "ds_read_b64_tr_b16 a[218:219], v232 offset:5632\n"
    "v_mfma_f32_16x16x32_bf16 a[92:95], a[252:255], v[148:151], a[92:95]\n"
    "v_mfma_f32_16x16x32_bf16 a[96:99], a[224:227], v[164:167], a[96:99]\n"
    "ds_read_b64_tr_b16 a[220:221], v233 offset:1536\n"
    "buffer_load_dwordx4 v[100:103], v234, s[44:47], s67 offen offset:1024 sc1\n"
    "v_mfma_f32_16x16x32_bf16 a[100:103], a[228:231], v[164:167], a[100:103]\n"
    "ds_read_b64_tr_b16 a[222:223], v233 offset:5632\n"
    "v_mfma_f32_16x16x32_bf16 a[104:107], a[232:235], v[164:167], a[104:107]\n"
    "v_mfma_f32_16x16x32_bf16 a[108:111], a[236:239], v[164:167], a[108:111]\n"
    "v_mfma_f32_16x16x32_bf16 a[112:115], a[240:243], v[164:167], a[112:115]\n"
    "v_mfma_f32_16x16x32_bf16 a[116:119], a[244:247], v[164:167], a[116:119]\n"
    "v_mfma_f32_16x16x32_bf16 a[120:123], a[248:251], v[164:167], a[120:123]\n"
    "v_mfma_f32_16x16x32_bf16 a[124:127], a[252:255], v[164:167], a[124:127]\n"
    "v_mfma_f32_16x16x32_bf16 a[128:131], a[224:227], v[180:183], a[128:131]\n"
    "v_mfma_f32_16x16x32_bf16 a[132:135], a[228:231], v[180:183], a[132:135]\n"
    "v_mfma_f32_16x16x32_bf16 a[136:139], a[232:235], v[180:183], a[136:139]\n"
    "v_mfma_f32_16x16x32_bf16 a[140:143], a[236:239], v[180:183], a[140:143]\n"
    "v_mfma_f32_16x16x32_bf16 a[144:147], a[240:243], v[180:183], a[144:147]\n"
    "v_mfma_f32_16x16x32_bf16 a[148:151], a[244:247], v[180:183], a[148:151]\n"
    "v_mfma_f32_16x16x32_bf16 a[152:155], a[248:251], v[180:183], a[152:155]\n"
    "v_mfma_f32_16x16x32_bf16 a[156:159], a[252:255], v[180:183], a[156:159]\n"
    "s_waitcnt lgkmcnt(0)\n"
    "s_waitcnt vmcnt(36)\n"
    "s_barrier\n"
    "s_waitcnt vmcnt(27)\n"
    "v_mfma_f32_16x16x32_bf16 a[0:3], a[192:195], v[120:123], a[0:3]\n"
    "v_add_u32 v232, 0x4000, v232\n"
    "v_mfma_f32_16x16x32_bf16 a[4:7], a[196:199], v[120:123], a[4:7]\n"
    "v_add_u32 v233, 0x4000, v233\n"
    "s_add_u32 s67, s66, 0\n"
    "v_mfma_f32_16x16x32_bf16 a[8:11], a[200:203], v[120:123], a[8:11]\n"
    "v_mfma_f32_16x16x32_bf16 a[12:15], a[204:207], v[120:123], a[12:15]\n"
    "ds_read_b64_tr_b16 a[224:225], v232 offset:0\n"
    "buffer_load_dwordx4 v[40:43], v234, s[44:47], s67 offen offset:2048 sc1\n"
    "v_mfma_f32_16x16x32_bf16 a[16:19], a[208:211], v[120:123], a[16:19]\n"
    "ds_read_b64_tr_b16 a[226:227], v232 offset:4096\n"
    "v_mfma_f32_16x16x32_bf16 a[20:23], a[212:215], v[120:123], a[20:23]\n"
    "v_mfma_f32_16x16x32_bf16 a[24:27], a[216:219], v[120:123], a[24:27]\n"
    "ds_read_b64_tr_b16 a[228:229], v233 offset:0\n"
    "s_add_u32 s67, s66, 4096\n"
    "v_mfma_f32_16x16x32_bf16 a[28:31], a[220:223], v[120:123], a[28:31]\n"
    "ds_read_b64_tr_b16 a[230:231], v233 offset:4096\n"
    "v_mfma_f32_16x16x32_bf16 a[32:35], a[192:195], v[136:139], a[32:35]\n"
    "buffer_load_dwordx4 v[56:59], v234, s[44:47], s67 offen offset:2048 sc1\n"
    "v_mfma_f32_16x16x32_bf16 a[36:39], a[196:199], v[136:139], a[36:39]\n"
    "ds_read_b64_tr_b16 a[232:233], v232 offset:512\n"
    "v_mfma_f32_16x16x32_bf16 a[40:43], a[200:203], v[136:139], a[40:43]\n"
    "ds_read_b64_tr_b16 a[234:235], v232 offset:4608\n"
    "v_mfma_f32_16x16x32_bf16 a[44:47], a[204:207], v[136:139], a[44:47]\n"
    "s_add_u32 s67, s66, 8192\n"
    "v_mfma_f32_16x16x32_bf16 a[48:51], a[208:211], v[136:139], a[48:51]\n"
    "ds_read_b64_tr_b16 a[236:237], v233 offset:512\n"
    "v_mfma_f32_16x16x32_bf16 a[52:55], a[212:215], v[136:139], a[52:55]\n"
    "ds_read_b64_tr_b16 a[238:239], v233 offset:4608\n"
    "v_mfma_f32_16x16x32_bf16 a[56:59], a[216:219], v[136:139], a[56:59]\n"
    "buffer_load_dwordx4 v[72:75], v234, s[44:47], s67 offen offset:2048 sc1\n"
    "v_mfma_f32_16x16x32_bf16 a[60:63], a[220:223], v[136:139], a[60:63]\n"
    "ds_read_b64_tr_b16 a[240:241], v232 offset:1024\n"
    "v_mfma_f32_16x16x32_bf16 a[64:67], a[192:195], v[152:155], a[64:67]\n"
    "ds_read_b64_tr_b16 a[242:243], v232 offset:5120\n"
    "s_add_u32 s67, s66, 12288\n"
    "v_mfma_f32_16x16x32_bf16 a[68:71], a[196:199], v[152:155], a[68:71]\n"
    "v_mfma_f32_16x16x32_bf16 a[72:75], a[200:203], v[152:155], a[72:75]\n"
    "ds_read_b64_tr_b16 a[244:245], v233 offset:1024\n"
    "v_mfma_f32_16x16x32_bf16 a[76:79], a[204:207], v[152:155], a[76:79]\n"
    "ds_read_b64_tr_b16 a[246:247], v233 offset:5120\n"
    "buffer_load_dwordx4 v[88:91], v234, s[44:47], s67 offen offset:2048 sc1\n"
    "v_mfma_f32_16x16x32_bf16 a[80:83], a[208:211], v[152:155], a[80:83]\n"
    "v_mfma_f32_16x16x32_bf16 a[84:87], a[212:215], v[152:155], a[84:87]\n"
    "ds_read_b64_tr_b16 a[248:249], v232 offset:1536\n"
    "s_add_u32 s67, s66, 16384\n"
    "v_mfma_f32_16x16x32_bf16 a[88:91], a[216:219], v[152:155], a[88:91]\n"
    "ds_read_b64_tr_b16 a[250:251], v232 offset:5632\n"
    "v_mfma_f32_16x16x32_bf16 a[92:95], a[220:223], v[152:155], a[92:95]\n"
    "v_mfma_f32_16x16x32_bf16 a[96:99], a[192:195], v[168:171], a[96:99]\n"
    "ds_read_b64_tr_b16 a[252:253], v233 offset:1536\n"
    "buffer_load_dwordx4 v[104:107], v234, s[44:47], s67 offen offset:2048 sc1\n"
    "v_mfma_f32_16x16x32_bf16 a[100:103], a[196:199], v[168:171], a[100:103]\n"
    "ds_read_b64_tr_b16 a[254:255], v233 offset:5632\n"
    "v_mfma_f32_16x16x32_bf16 a[104:107], a[200:203], v[168:171], a[104:107]\n"
    "v_mfma_f32_16x16x32_bf16 a[108:111], a[204:207], v[168:171], a[108:111]\n"
    "v_mfma_f32_16x16x32_bf16 a[112:115], a[208:211], v[168:171], a[112:115]\n"
    "v_mfma_f32_16x16x32_bf16 a[116:119], a[212:215], v[168:171], a[116:119]\n"
    "v_mfma_f32_16x16x32_bf16 a[120:123], a[216:219], v[168:171], a[120:123]\n"
    "v_mfma_f32_16x16x32_bf16 a[124:127], a[220:223], v[168:171], a[124:127]\n"
    "v_mfma_f32_16x16x32_bf16 a[128:131], a[192:195], v[184:187], a[128:131]\n"
    "v_mfma_f32_16x16x32_bf16 a[132:135], a[196:199], v[184:187], a[132:135]\n"
    "v_mfma_f32_16x16x32_bf16 a[136:139], a[200:203], v[184:187], a[136:139]\n"
    "v_mfma_f32_16x16x32_bf16 a[140:143], a[204:207], v[184:187], a[140:143]\n"
    "v_mfma_f32_16x16x32_bf16 a[144:147], a[208:211], v[184:187], a[144:147]\n"
    "v_mfma_f32_16x16x32_bf16 a[148:151], a[212:215], v[184:187], a[148:151]\n"
    "v_mfma_f32_16x16x32_bf16 a[152:155], a[216:219], v[184:187], a[152:155]\n"
    "v_mfma_f32_16x16x32_bf16 a[156:159], a[220:223], v[184:187], a[156:159]\n"
    "s_waitcnt lgkmcnt(0)\n"
    "s_waitcnt vmcnt(32)\n"
    "s_barrier\n"
    "s_waitcnt vmcnt(23)\n"
    "v_mfma_f32_16x16x32_bf16 a[0:3], a[224:227], v[124:127], a[0:3]\n"
    "v_add_u32 v232, 0xfffdc000, v232\n"
    "v_mfma_f32_16x16x32_bf16 a[4:7], a[228:231], v[124:127], a[4:7]\n"
    "v_add_u32 v233, 0xfffdc000, v233\n"
    "s_add_u32 s67, s66, 0\n"
    "v_mfma_f32_16x16x32_bf16 a[8:11], a[232:235], v[124:127], a[8:11]\n"
    "v_mfma_f32_16x16x32_bf16 a[12:15], a[236:239], v[124:127], a[12:15]\n"
    "ds_read_b64_tr_b16 a[192:193], v232 offset:0\n"
    "buffer_load_dwordx4 v[44:47], v234, s[44:47], s67 offen offset:3072 sc1\n"
    "v_mfma_f32_16x16x32_bf16 a[16:19], a[240:243], v[124:127], a[16:19]\n"
    "ds_read_b64_tr_b16 a[194:195], v232 offset:4096\n"
    "v_mfma_f32_16x16x32_bf16 a[20:23], a[244:247], v[124:127], a[20:23]\n"
    "v_mfma_f32_16x16x32_bf16 a[24:27], a[248:251], v[124:127], a[24:27]\n"
    "ds_read_b64_tr_b16 a[196:197], v233 offset:0\n"
    "s_add_u32 s67, s66, 4096\n"
    "v_mfma_f32_16x16x32_bf16 a[28:31], a[252:255], v[124:127], a[28:31]\n"
    "ds_read_b64_tr_b16 a[198:199], v233 offset:4096\n"
    "v_mfma_f32_16x16x32_bf16 a[32:35], a[224:227], v[140:143], a[32:35]\n"
    "buffer_load_dwordx4 v[60:63], v234, s[44:47], s67 offen offset:3072 sc1\n"
    "v_mfma_f32_16x16x32_bf16 a[36:39], a[228:231], v[140:143], a[36:39]\n"
    "ds_read_b64_tr_b16 a[200:201], v232 offset:512\n"
    "v_mfma_f32_16x16x32_bf16 a[40:43], a[232:235], v[140:143], a[40:43]\n"
    "ds_read_b64_tr_b16 a[202:203], v232 offset:4608\n"
    "v_mfma_f32_16x16x32_bf16 a[44:47], a[236:239], v[140:143], a[44:47]\n"
    "s_add_u32 s67, s66, 8192\n"
    "v_mfma_f32_16x16x32_bf16 a[48:51], a[240:243], v[140:143], a[48:51]\n"
    "ds_read_b64_tr_b16 a[204:205], v233 offset:512\n"
    "v_mfma_f32_16x16x32_bf16 a[52:55], a[244:247], v[140:143], a[52:55]\n"
    "ds_read_b64_tr_b16 a[206:207], v233 offset:4608\n"
    "v_mfma_f32_16x16x32_bf16 a[56:59], a[248:251], v[140:143], a[56:59]\n"
    "buffer_load_dwordx4 v[76:79], v234, s[44:47], s67 offen offset:3072 sc1\n"
    "v_mfma_f32_16x16x32_bf16 a[60:63], a[252:255], v[140:143], a[60:63]\n"
    "ds_read_b64_tr_b16 a[208:209], v232 offset:1024\n"
    "v_mfma_f32_16x16x32_bf16 a[64:67], a[224:227], v[156:159], a[64:67]\n"
    "ds_read_b64_tr_b16 a[210:211], v232 offset:5120\n"
    "s_add_u32 s67, s66, 12288\n"
    "v_mfma_f32_16x16x32_bf16 a[68:71], a[228:231], v[156:159], a[68:71]\n"
    "v_mfma_f32_16x16x32_bf16 a[72:75], a[232:235], v[156:159], a[72:75]\n"
    "ds_read_b64_tr_b16 a[212:213], v233 offset:1024\n"
    "v_mfma_f32_16x16x32_bf16 a[76:79], a[236:239], v[156:159], a[76:79]\n"
    "ds_read_b64_tr_b16 a[214:215], v233 offset:5120\n"
    "buffer_load_dwordx4 v[92:95], v234, s[44:47], s67 offen offset:3072 sc1\n"
    "v_mfma_f32_16x16x32_bf16 a[80:83], a[240:243], v[156:159], a[80:83]\n"
    "v_mfma_f32_16x16x32_bf16 a[84:87], a[244:247], v[156:159], a[84:87]\n"
    "ds_read_b64_tr_b16 a[216:217], v232 offset:1536\n"
    "s_add_u32 s67, s66, 16384\n"
    "v_mfma_f32_16x16x32_bf16 a[88:91], a[248:251], v[156:159], a[88:91]\n"
    "ds_read_b64_tr_b16 a[218:219], v232 offset:5632\n"
    "v_mfma_f32_16x16x32_bf16 a[92:95], a[252:255], v[156:159], a[92:95]\n"
    "v_mfma_f32_16x16x32_bf16 a[96:99], a[224:227], v[172:175], a[96:99]\n"
    "ds_read_b64_tr_b16 a[220:221], v233 offset:1536\n"
    "buffer_load_dwordx4 v[108:111], v234, s[44:47], s67 offen offset:3072 sc1\n"
    "v_mfma_f32_16x16x32_bf16 a[100:103], a[228:231], v[172:175], a[100:103]\n"
    "ds_read_b64_tr_b16 a[222:223], v233 offset:5632\n"
    "v_mfma_f32_16x16x32_bf16 a[104:107], a[232:235], v[172:175], a[104:107]\n"
    "v_mfma_f32_16x16x32_bf16 a[108:111], a[236:239], v[172:175], a[108:111]\n"
    "v_mfma_f32_16x16x32_bf16 a[112:115], a[240:243], v[172:175], a[112:115]\n"
    "v_mfma_f32_16x16x32_bf16 a[116:119], a[244:247], v[172:175], a[116:119]\n"
    "v_mfma_f32_16x16x32_bf16 a[120:123], a[248:251], v[172:175], a[120:123]\n"
    "v_mfma_f32_16x16x32_bf16 a[124:127], a[252:255], v[172:175], a[124:127]\n"
    "v_mfma_f32_16x16x32_bf16 a[128:131], a[224:227], v[188:191], a[128:131]\n"
    "v_mfma_f32_16x16x32_bf16 a[132:135], a[228:231], v[188:191], a[132:135]\n"
    "v_mfma_f32_16x16x32_bf16 a[136:139], a[232:235], v[188:191], a[136:139]\n"
    "v_mfma_f32_16x16x32_bf16 a[140:143], a[236:239], v[188:191], a[140:143]\n"
    "v_mfma_f32_16x16x32_bf16 a[144:147], a[240:243], v[188:191], a[144:147]\n"
    "v_mfma_f32_16x16x32_bf16 a[148:151], a[244:247], v[188:191], a[148:151]\n"
    "v_mfma_f32_16x16x32_bf16 a[152:155], a[248:251], v[188:191], a[152:155]\n"
    "v_mfma_f32_16x16x32_bf16 a[156:159], a[252:255], v[188:191], a[156:159]\n"
    "s_waitcnt lgkmcnt(0)\n"
    "s_waitcnt vmcnt(28)\n"
    "s_barrier\n"
    "s_waitcnt vmcnt(19)\n"
    "v_mfma_f32_16x16x32_bf16 a[0:3], a[192:195], v[32:35], a[0:3]\n"
    "v_add_u32 v232, 0x4000, v232\n"
    "v_mfma_f32_16x16x32_bf16 a[4:7], a[196:199], v[32:35], a[4:7]\n"
    "v_add_u32 v233, 0x4000, v233\n"
    "v_mfma_f32_16x16x32_bf16 a[8:11], a[200:203], v[32:35], a[8:11]\n"
    "v_mfma_f32_16x16x32_bf16 a[12:15], a[204:207], v[32:35], a[12:15]\n"
    "ds_read_b64_tr_b16 a[224:225], v232 offset:0\n"
    "v_mfma_f32_16x16x32_bf16 a[16:19], a[208:211], v[32:35], a[16:19]\n"
    "ds_read_b64_tr_b16 a[226:227], v232 offset:4096\n"
    "v_mfma_f32_16x16x32_bf16 a[20:23], a[212:215], v[32:35], a[20:23]\n"
    "v_mfma_f32_16x16x32_bf16 a[24:27], a[216:219], v[32:35], a[24:27]\n"
    "ds_read_b64_tr_b16 a[228:229], v233 offset:0\n"
    "v_mfma_f32_16x16x32_bf16 a[28:31], a[220:223], v[32:35], a[28:31]\n"
    "ds_read_b64_tr_b16 a[230:231], v233 offset:4096\n"
    "v_mfma_f32_16x16x32_bf16 a[32:35], a[192:195], v[48:51], a[32:35]\n"
    "v_mfma_f32_16x16x32_bf16 a[36:39], a[196:199], v[48:51], a[36:39]\n"
    "ds_read_b64_tr_b16 a[232:233], v232 offset:512\n"
    "v_mfma_f32_16x16x32_bf16 a[40:43], a[200:203], v[48:51], a[40:43]\n"
    "ds_read_b64_tr_b16 a[234:235], v232 offset:4608\n"
    "v_mfma_f32_16x16x32_bf16 a[44:47], a[204:207], v[48:51], a[44:47]\n"
    "v_mfma_f32_16x16x32_bf16 a[48:51], a[208:211], v[48:51], a[48:51]\n"
    "ds_read_b64_tr_b16 a[236:237], v233 offset:512\n"
    "v_mfma_f32_16x16x32_bf16 a[52:55], a[212:215], v[48:51], a[52:55]\n"
    "ds_read_b64_tr_b16 a[238:239], v233 offset:4608\n"
    "v_mfma_f32_16x16x32_bf16 a[56:59], a[216:219], v[48:51], a[56:59]\n"
    "v_mfma_f32_16x16x32_bf16 a[60:63], a[220:223], v[48:51], a[60:63]\n"
    "ds_read_b64_tr_b16 a[240:241], v232 offset:1024\n"
    "v_mfma_f32_16x16x32_bf16 a[64:67], a[192:195], v[64:67], a[64:67]\n"
    "ds_read_b64_tr_b16 a[242:243], v232 offset:5120\n"
    "v_mfma_f32_16x16x32_bf16 a[68:71], a[196:199], v[64:67], a[68:71]\n"
    "v_mfma_f32_16x16x32_bf16 a[72:75], a[200:203], v[64:67], a[72:75]\n"
    "ds_read_b64_tr_b16 a[244:245], v233 offset:1024\n"
    "v_mfma_f32_16x16x32_bf16 a[76:79], a[204:207], v[64:67], a[76:79]\n"
    "ds_read_b64_tr_b16 a[246:247], v233 offset:5120\n"
    "v_mfma_f32_16x16x32_bf16 a[80:83], a[208:211], v[64:67], a[80:83]\n"
    "v_mfma_f32_16x16x32_bf16 a[84:87], a[212:215], v[64:67], a[84:87]\n"
    "ds_read_b64_tr_b16 a[248:249], v232 offset:1536\n"
    "v_mfma_f32_16x16x32_bf16 a[88:91], a[216:219], v[64:67], a[88:91]\n"
    "ds_read_b64_tr_b16 a[250:251], v232 offset:5632\n"
    "v_mfma_f32_16x16x32_bf16 a[92:95], a[220:223], v[64:67], a[92:95]\n"
    "v_mfma_f32_16x16x32_bf16 a[96:99], a[192:195], v[80:83], a[96:99]\n"
    "ds_read_b64_tr_b16 a[252:253], v233 offset:1536\n"
    "v_mfma_f32_16x16x32_bf16 a[100:103], a[196:199], v[80:83], a[100:103]\n"
    "ds_read_b64_tr_b16 a[254:255], v233 offset:5632\n"
    "v_mfma_f32_16x16x32_bf16 a[104:107], a[200:203], v[80:83], a[104:107]\n"
    "v_mfma_f32_16x16x32_bf16 a[108:111], a[204:207], v[80:83], a[108:111]\n"
    "v_mfma_f32_16x16x32_bf16 a[112:115], a[208:211], v[80:83], a[112:115]\n"
    "v_mfma_f32_16x16x32_bf16 a[116:119], a[212:215], v[80:83], a[116:119]\n"
    "v_mfma_f32_16x16x32_bf16 a[120:123], a[216:219], v[80:83], a[120:123]\n"
    "v_mfma_f32_16x16x32_bf16 a[124:127], a[220:223], v[80:83], a[124:127]\n"
    "v_mfma_f32_16x16x32_bf16 a[128:131], a[192:195], v[96:99], a[128:131]\n"
    "v_mfma_f32_16x16x32_bf16 a[132:135], a[196:199], v[96:99], a[132:135]\n"
    "v_mfma_f32_16x16x32_bf16 a[136:139], a[200:203], v[96:99], a[136:139]\n"
    "v_mfma_f32_16x16x32_bf16 a[140:143], a[204:207], v[96:99], a[140:143]\n"
    "v_mfma_f32_16x16x32_bf16 a[144:147], a[208:211], v[96:99], a[144:147]\n"
    "v_mfma_f32_16x16x32_bf16 a[148:151], a[212:215], v[96:99], a[148:151]\n"
    "v_mfma_f32_16x16x32_bf16 a[152:155], a[216:219], v[96:99], a[152:155]\n"
    "v_mfma_f32_16x16x32_bf16 a[156:159], a[220:223], v[96:99], a[156:159]\n"
    "s_waitcnt lgkmcnt(0)\n"
    "s_waitcnt vmcnt(19)\n"
    "s_barrier\n"
    "s_waitcnt vmcnt(10)\n"
    "v_mfma_f32_16x16x32_bf16 a[0:3], a[224:227], v[36:39], a[0:3]\n"
    "v_add_u32 v232, 0x4000, v232\n"
    "v_mfma_f32_16x16x32_bf16 a[4:7], a[228:231], v[36:39], a[4:7]\n"
    "v_add_u32 v233, 0x4000, v233\n"
    "v_mfma_f32_16x16x32_bf16 a[8:11], a[232:235], v[36:39], a[8:11]\n"
    "v_mfma_f32_16x16x32_bf16 a[12:15], a[236:239], v[36:39], a[12:15]\n"
    "ds_read_b64_tr_b16 a[192:193], v232 offset:0\n"
    "v_mfma_f32_16x16x32_bf16 a[16:19], a[240:243], v[36:39], a[16:19]\n"
    "ds_read_b64_tr_b16 a[194:195], v232 offset:4096\n"
    "v_mfma_f32_16x16x32_bf16 a[20:23], a[244:247], v[36:39], a[20:23]\n"
    "v_mfma_f32_16x16x32_bf16 a[24:27], a[248:251], v[36:39], a[24:27]\n"
    "ds_read_b64_tr_b16 a[196:197], v233 offset:0\n"
    "v_mfma_f32_16x16x32_bf16 a[28:31], a[252:255], v[36:39], a[28:31]\n"
    "ds_read_b64_tr_b16 a[198:199], v233 offset:4096\n"
    "v_mfma_f32_16x16x32_bf16 a[32:35], a[224:227], v[52:55], a[32:35]\n"
    "v_mfma_f32_16x16x32_bf16 a[36:39], a[228:231], v[52:55], a[36:39]\n"
    "ds_read_b64_tr_b16 a[200:201], v232 offset:512\n"
    "v_mfma_f32_16x16x32_bf16 a[40:43], a[232:235], v[52:55], a[40:43]\n"
    "ds_read_b64_tr_b16 a[202:203], v232 offset:4608\n"
    "v_mfma_f32_16x16x32_bf16 a[44:47], a[236:239], v[52:55], a[44:47]\n"
    "v_mfma_f32_16x16x32_bf16 a[48:51], a[240:243], v[52:55], a[48:51]\n"
    "ds_read_b64_tr_b16 a[204:205], v233 offset:512\n"
    "v_mfma_f32_16x16x32_bf16 a[52:55], a[244:247], v[52:55], a[52:55]\n"
    "ds_read_b64_tr_b16 a[206:207], v233 offset:4608\n"
    "v_mfma_f32_16x16x32_bf16 a[56:59], a[248:251], v[52:55], a[56:59]\n"
    "v_mfma_f32_16x16x32_bf16 a[60:63], a[252:255], v[52:55], a[60:63]\n"
    "ds_read_b64_tr_b16 a[208:209], v232 offset:1024\n"
    "v_mfma_f32_16x16x32_bf16 a[64:67], a[224:227], v[68:71], a[64:67]\n"
    "ds_read_b64_tr_b16 a[210:211], v232 offset:5120\n"
    "v_mfma_f32_16x16x32_bf16 a[68:71], a[228:231], v[68:71], a[68:71]\n"
    "v_mfma_f32_16x16x32_bf16 a[72:75], a[232:235], v[68:71], a[72:75]\n"
    "ds_read_b64_tr_b16 a[212:213], v233 offset:1024\n"
    "v_mfma_f32_16x16x32_bf16 a[76:79], a[236:239], v[68:71], a[76:79]\n"
    "ds_read_b64_tr_b16 a[214:215], v233 offset:5120\n"
    "v_mfma_f32_16x16x32_bf16 a[80:83], a[240:243], v[68:71], a[80:83]\n"
    "v_mfma_f32_16x16x32_bf16 a[84:87], a[244:247], v[68:71], a[84:87]\n"
    "ds_read_b64_tr_b16 a[216:217], v232 offset:1536\n"
    "v_mfma_f32_16x16x32_bf16 a[88:91], a[248:251], v[68:71], a[88:91]\n"
    "ds_read_b64_tr_b16 a[218:219], v232 offset:5632\n"
    "v_mfma_f32_16x16x32_bf16 a[92:95], a[252:255], v[68:71], a[92:95]\n"
    "v_mfma_f32_16x16x32_bf16 a[96:99], a[224:227], v[84:87], a[96:99]\n"
    "ds_read_b64_tr_b16 a[220:221], v233 offset:1536\n"
    "v_mfma_f32_16x16x32_bf16 a[100:103], a[228:231], v[84:87], a[100:103]\n"
    "ds_read_b64_tr_b16 a[222:223], v233 offset:5632\n"
    "v_mfma_f32_16x16x32_bf16 a[104:107], a[232:235], v[84:87], a[104:107]\n"
    "v_mfma_f32_16x16x32_bf16 a[108:111], a[236:239], v[84:87], a[108:111]\n"
    "v_mfma_f32_16x16x32_bf16 a[112:115], a[240:243], v[84:87], a[112:115]\n"
    "v_mfma_f32_16x16x32_bf16 a[116:119], a[244:247], v[84:87], a[116:119]\n"
    "v_mfma_f32_16x16x32_bf16 a[120:123], a[248:251], v[84:87], a[120:123]\n"
    "v_mfma_f32_16x16x32_bf16 a[124:127], a[252:255], v[84:87], a[124:127]\n"
    "v_mfma_f32_16x16x32_bf16 a[128:131], a[224:227], v[100:103], a[128:131]\n"
    "v_mfma_f32_16x16x32_bf16 a[132:135], a[228:231], v[100:103], a[132:135]\n"
    "v_mfma_f32_16x16x32_bf16 a[136:139], a[232:235], v[100:103], a[136:139]\n"
    "v_mfma_f32_16x16x32_bf16 a[140:143], a[236:239], v[100:103], a[140:143]\n"
    "v_mfma_f32_16x16x32_bf16 a[144:147], a[240:243], v[100:103], a[144:147]\n"
    "v_mfma_f32_16x16x32_bf16 a[148:151], a[244:247], v[100:103], a[148:151]\n"
    "v_mfma_f32_16x16x32_bf16 a[152:155], a[248:251], v[100:103], a[152:155]\n"
    "v_mfma_f32_16x16x32_bf16 a[156:159], a[252:255], v[100:103], a[156:159]\n"
    "s_waitcnt lgkmcnt(0)\n"
    "s_waitcnt vmcnt(10)\n"
    "s_barrier\n"
    "s_waitcnt vmcnt(5)\n"
    "v_mfma_f32_16x16x32_bf16 a[0:3], a[192:195], v[40:43], a[0:3]\n"
    "v_add_u32 v232, 0x4000, v232\n"
    "v_mfma_f32_16x16x32_bf16 a[4:7], a[196:199], v[40:43], a[4:7]\n"
    "v_add_u32 v233, 0x4000, v233\n"
    "v_mfma_f32_16x16x32_bf16 a[8:11], a[200:203], v[40:43], a[8:11]\n"
    "v_mfma_f32_16x16x32_bf16 a[12:15], a[204:207], v[40:43], a[12:15]\n"
    "ds_read_b64_tr_b16 a[224:225], v232 offset:0\n"
    "v_mfma_f32_16x16x32_bf16 a[16:19], a[208:211], v[40:43], a[16:19]\n"
    "ds_read_b64_tr_b16 a[226:227], v232 offset:4096\n"
    "v_mfma_f32_16x16x32_bf16 a[20:23], a[212:215], v[40:43], a[20:23]\n"
    "v_mfma_f32_16x16x32_bf16 a[24:27], a[216:219], v[40:43], a[24:27]\n"
    "ds_read_b64_tr_b16 a[228:229], v233 offset:0\n"
    "v_mfma_f32_16x16x32_bf16 a[28:31], a[220:223], v[40:43], a[28:31]\n"
    "ds_read_b64_tr_b16 a[230:231], v233 offset:4096\n"
    "v_mfma_f32_16x16x32_bf16 a[32:35], a[192:195], v[56:59], a[32:35]\n"
    "v_mfma_f32_16x16x32_bf16 a[36:39], a[196:199], v[56:59], a[36:39]\n"
    "ds_read_b64_tr_b16 a[232:233], v232 offset:512\n"
    "v_mfma_f32_16x16x32_bf16 a[40:43], a[200:203], v[56:59], a[40:43]\n"
    "ds_read_b64_tr_b16 a[234:235], v232 offset:4608\n"
    "v_mfma_f32_16x16x32_bf16 a[44:47], a[204:207], v[56:59], a[44:47]\n"
    "v_mfma_f32_16x16x32_bf16 a[48:51], a[208:211], v[56:59], a[48:51]\n"
    "ds_read_b64_tr_b16 a[236:237], v233 offset:512\n"
    "v_mfma_f32_16x16x32_bf16 a[52:55], a[212:215], v[56:59], a[52:55]\n"
    "ds_read_b64_tr_b16 a[238:239], v233 offset:4608\n"
    "v_mfma_f32_16x16x32_bf16 a[56:59], a[216:219], v[56:59], a[56:59]\n"
    "v_mfma_f32_16x16x32_bf16 a[60:63], a[220:223], v[56:59], a[60:63]\n"
    "ds_read_b64_tr_b16 a[240:241], v232 offset:1024\n"
    "v_mfma_f32_16x16x32_bf16 a[64:67], a[192:195], v[72:75], a[64:67]\n"
    "ds_read_b64_tr_b16 a[242:243], v232 offset:5120\n"
    "v_mfma_f32_16x16x32_bf16 a[68:71], a[196:199], v[72:75], a[68:71]\n"
    "v_mfma_f32_16x16x32_bf16 a[72:75], a[200:203], v[72:75], a[72:75]\n"
    "ds_read_b64_tr_b16 a[244:245], v233 offset:1024\n"
    "v_mfma_f32_16x16x32_bf16 a[76:79], a[204:207], v[72:75], a[76:79]\n"
    "ds_read_b64_tr_b16 a[246:247], v233 offset:5120\n"
    "v_mfma_f32_16x16x32_bf16 a[80:83], a[208:211], v[72:75], a[80:83]\n"
    "v_mfma_f32_16x16x32_bf16 a[84:87], a[212:215], v[72:75], a[84:87]\n"
    "ds_read_b64_tr_b16 a[248:249], v232 offset:1536\n"
    "v_mfma_f32_16x16x32_bf16 a[88:91], a[216:219], v[72:75], a[88:91]\n"
    "ds_read_b64_tr_b16 a[250:251], v232 offset:5632\n"
    "v_mfma_f32_16x16x32_bf16 a[92:95], a[220:223], v[72:75], a[92:95]\n"
    "v_mfma_f32_16x16x32_bf16 a[96:99], a[192:195], v[88:91], a[96:99]\n"
    "ds_read_b64_tr_b16 a[252:253], v233 offset:1536\n"
    "v_mfma_f32_16x16x32_bf16 a[100:103], a[196:199], v[88:91], a[100:103]\n"
    "ds_read_b64_tr_b16 a[254:255], v233 offset:5632\n"
    "v_mfma_f32_16x16x32_bf16 a[104:107], a[200:203], v[88:91], a[104:107]\n"
    "v_mfma_f32_16x16x32_bf16 a[108:111], a[204:207], v[88:91], a[108:111]\n"
    "v_mfma_f32_16x16x32_bf16 a[112:115], a[208:211], v[88:91], a[112:115]\n"
    "v_mfma_f32_16x16x32_bf16 a[116:119], a[212:215], v[88:91], a[116:119]\n"
    "v_mfma_f32_16x16x32_bf16 a[120:123], a[216:219], v[88:91], a[120:123]\n"
    "v_mfma_f32_16x16x32_bf16 a[124:127], a[220:223], v[88:91], a[124:127]\n"
    "v_mfma_f32_16x16x32_bf16 a[128:131], a[192:195], v[104:107], a[128:131]\n"
    "v_mfma_f32_16x16x32_bf16 a[132:135], a[196:199], v[104:107], a[132:135]\n"
    "v_mfma_f32_16x16x32_bf16 a[136:139], a[200:203], v[104:107], a[136:139]\n"
    "v_mfma_f32_16x16x32_bf16 a[140:143], a[204:207], v[104:107], a[140:143]\n"
    "v_mfma_f32_16x16x32_bf16 a[144:147], a[208:211], v[104:107], a[144:147]\n"
    "v_mfma_f32_16x16x32_bf16 a[148:151], a[212:215], v[104:107], a[148:151]\n"
    "v_mfma_f32_16x16x32_bf16 a[152:155], a[216:219], v[104:107], a[152:155]\n"
    "v_mfma_f32_16x16x32_bf16 a[156:159], a[220:223], v[104:107], a[156:159]\n"
    "s_waitcnt lgkmcnt(0)\n"
    "s_barrier\n"
    "s_waitcnt vmcnt(0)\n"
    "v_mfma_f32_16x16x32_bf16 a[0:3], a[224:227], v[44:47], a[0:3]\n"
    "v_mfma_f32_16x16x32_bf16 a[4:7], a[228:231], v[44:47], a[4:7]\n"
    "v_mfma_f32_16x16x32_bf16 a[8:11], a[232:235], v[44:47], a[8:11]\n"
    "v_mfma_f32_16x16x32_bf16 a[12:15], a[236:239], v[44:47], a[12:15]\n"
    "v_mfma_f32_16x16x32_bf16 a[16:19], a[240:243], v[44:47], a[16:19]\n"
    "v_mfma_f32_16x16x32_bf16 a[20:23], a[244:247], v[44:47], a[20:23]\n"
    "v_mfma_f32_16x16x32_bf16 a[24:27], a[248:251], v[44:47], a[24:27]\n"
    "v_mfma_f32_16x16x32_bf16 a[28:31], a[252:255], v[44:47], a[28:31]\n"
    "v_mfma_f32_16x16x32_bf16 a[32:35], a[224:227], v[60:63], a[32:35]\n"
    "v_mfma_f32_16x16x32_bf16 a[36:39], a[228:231], v[60:63], a[36:39]\n"
    "v_mfma_f32_16x16x32_bf16 a[40:43], a[232:235], v[60:63], a[40:43]\n"
    "v_mfma_f32_16x16x32_bf16 a[44:47], a[236:239], v[60:63], a[44:47]\n"
    "v_mfma_f32_16x16x32_bf16 a[48:51], a[240:243], v[60:63], a[48:51]\n"
    "v_mfma_f32_16x16x32_bf16 a[52:55], a[244:247], v[60:63], a[52:55]\n"
    "v_mfma_f32_16x16x32_bf16 a[56:59], a[248:251], v[60:63], a[56:59]\n"
    "v_mfma_f32_16x16x32_bf16 a[60:63], a[252:255], v[60:63], a[60:63]\n"
    "v_mfma_f32_16x16x32_bf16 a[64:67], a[224:227], v[76:79], a[64:67]\n"
    "v_mfma_f32_16x16x32_bf16 a[68:71], a[228:231], v[76:79], a[68:71]\n"
    "v_mfma_f32_16x16x32_bf16 a[72:75], a[232:235], v[76:79], a[72:75]\n"
    "v_mfma_f32_16x16x32_bf16 a[76:79], a[236:239], v[76:79], a[76:79]\n"
    "v_mfma_f32_16x16x32_bf16 a[80:83], a[240:243], v[76:79], a[80:83]\n"
    "v_mfma_f32_16x16x32_bf16 a[84:87], a[244:247], v[76:79], a[84:87]\n"
    "v_mfma_f32_16x16x32_bf16 a[88:91], a[248:251], v[76:79], a[88:91]\n"
    "v_mfma_f32_16x16x32_bf16 a[92:95], a[252:255], v[76:79], a[92:95]\n"
    "v_mfma_f32_16x16x32_bf16 a[96:99], a[224:227], v[92:95], a[96:99]\n"
    "v_mfma_f32_16x16x32_bf16 a[100:103], a[228:231], v[92:95], a[100:103]\n"
    "v_mfma_f32_16x16x32_bf16 a[104:107], a[232:235], v[92:95], a[104:107]\n"
    "v_mfma_f32_16x16x32_bf16 a[108:111], a[236:239], v[92:95], a[108:111]\n"
    "v_mfma_f32_16x16x32_bf16 a[112:115], a[240:243], v[92:95], a[112:115]\n"
    "v_mfma_f32_16x16x32_bf16 a[116:119], a[244:247], v[92:95], a[116:119]\n"
    "v_mfma_f32_16x16x32_bf16 a[120:123], a[248:251], v[92:95], a[120:123]\n"
    "v_mfma_f32_16x16x32_bf16 a[124:127], a[252:255], v[92:95], a[124:127]\n"
    "v_mfma_f32_16x16x32_bf16 a[128:131], a[224:227], v[108:111], a[128:131]\n"
    "v_mfma_f32_16x16x32_bf16 a[132:135], a[228:231], v[108:111], a[132:135]\n"
    "v_mfma_f32_16x16x32_bf16 a[136:139], a[232:235], v[108:111], a[136:139]\n"
    "v_mfma_f32_16x16x32_bf16 a[140:143], a[236:239], v[108:111], a[140:143]\n"
    "v_mfma_f32_16x16x32_bf16 a[144:147], a[240:243], v[108:111], a[144:147]\n"
    "v_mfma_f32_16x16x32_bf16 a[148:151], a[244:247], v[108:111], a[148:151]\n"
    "v_mfma_f32_16x16x32_bf16 a[152:155], a[248:251], v[108:111], a[152:155]\n"
    "v_mfma_f32_16x16x32_bf16 a[156:159], a[252:255], v[108:111], a[156:159]\n"
    "s_mul_i32 s64, s62, 81920\n"
    "s_mul_i32 s65, s58, 20480\n"
    "s_add_u32 s64, s64, s65\n"
    "v_lshlrev_b32 v235, 3, v1\n"
    "v_accvgpr_read_b32 v32, a0\n"
    "v_accvgpr_read_b32 v33, a1\n"
    "v_accvgpr_read_b32 v34, a2\n"
    "v_accvgpr_read_b32 v35, a3\n"
    "v_accvgpr_read_b32 v36, a32\n"
    "v_accvgpr_read_b32 v37, a33\n"
    "v_accvgpr_read_b32 v38, a34\n"
    "v_accvgpr_read_b32 v39, a35\n"
    "v_accvgpr_read_b32 v40, a64\n"
    "v_accvgpr_read_b32 v41, a65\n"
    "v_accvgpr_read_b32 v42, a66\n"
    "v_accvgpr_read_b32 v43, a67\n"
    "v_accvgpr_read_b32 v44, a96\n"
    "v_accvgpr_read_b32 v45, a97\n"
    "v_accvgpr_read_b32 v46, a98\n"
    "v_accvgpr_read_b32 v47, a99\n"
    "v_accvgpr_read_b32 v48, a128\n"
    "v_accvgpr_read_b32 v49, a129\n"
    "v_accvgpr_read_b32 v50, a130\n"
    "v_accvgpr_read_b32 v51, a131\n"
    "v_cvt_pk_bf16_f32 v128, v32, v33\n"
    "v_cvt_pk_bf16_f32 v129, v34, v35\n"
    "v_cvt_pk_bf16_f32 v130, v36, v37\n"
    "v_cvt_pk_bf16_f32 v131, v38, v39\n"
    "v_cvt_pk_bf16_f32 v132, v40, v41\n"
    "v_cvt_pk_bf16_f32 v133, v42, v43\n"
    "v_cvt_pk_bf16_f32 v134, v44, v45\n"
    "v_cvt_pk_bf16_f32 v135, v46, v47\n"
    "v_cvt_pk_bf16_f32 v136, v48, v49\n"
    "v_cvt_pk_bf16_f32 v137, v50, v51\n"
    "s_add_u32 s65, s64, 0\n"
    "buffer_store_dwordx4 v[128:131], v234, s[48:51], s65 offen sc1\n"
    "buffer_store_dwordx4 v[132:135], v234, s[48:51], s65 offen offset:1024 sc1\n"
    "buffer_store_dwordx2 v[136:137], v235, s[48:51], s65 offen offset:2048 sc1\n"
    "v_accvgpr_read_b32 v56, a4\n"
    "v_accvgpr_read_b32 v57, a5\n"
    "v_accvgpr_read_b32 v58, a6\n"
    "v_accvgpr_read_b32 v59, a7\n"
    "v_accvgpr_read_b32 v60, a36\n"
    "v_accvgpr_read_b32 v61, a37\n"
    "v_accvgpr_read_b32 v62, a38\n"
    "v_accvgpr_read_b32 v63, a39\n"
    "v_accvgpr_read_b32 v64, a68\n"
    "v_accvgpr_read_b32 v65, a69\n"
    "v_accvgpr_read_b32 v66, a70\n"
    "v_accvgpr_read_b32 v67, a71\n"
    "v_accvgpr_read_b32 v68, a100\n"
    "v_accvgpr_read_b32 v69, a101\n"
    "v_accvgpr_read_b32 v70, a102\n"
    "v_accvgpr_read_b32 v71, a103\n"
    "v_accvgpr_read_b32 v72, a132\n"
    "v_accvgpr_read_b32 v73, a133\n"
    "v_accvgpr_read_b32 v74, a134\n"
    "v_accvgpr_read_b32 v75, a135\n"
    "v_cvt_pk_bf16_f32 v140, v56, v57\n"
    "v_cvt_pk_bf16_f32 v141, v58, v59\n"
    "v_cvt_pk_bf16_f32 v142, v60, v61\n"
    "v_cvt_pk_bf16_f32 v143, v62, v63\n"
    "v_cvt_pk_bf16_f32 v144, v64, v65\n"
    "v_cvt_pk_bf16_f32 v145, v66, v67\n"
    "v_cvt_pk_bf16_f32 v146, v68, v69\n"
    "v_cvt_pk_bf16_f32 v147, v70, v71\n"
    "v_cvt_pk_bf16_f32 v148, v72, v73\n"
    "v_cvt_pk_bf16_f32 v149, v74, v75\n"
    "s_add_u32 s65, s64, 2560\n"
    "buffer_store_dwordx4 v[140:143], v234, s[48:51], s65 offen sc1\n"
    "buffer_store_dwordx4 v[144:147], v234, s[48:51], s65 offen offset:1024 sc1\n"
    "buffer_store_dwordx2 v[148:149], v235, s[48:51], s65 offen offset:2048 sc1\n"
    "v_accvgpr_read_b32 v80, a8\n"
    "v_accvgpr_read_b32 v81, a9\n"
    "v_accvgpr_read_b32 v82, a10\n"
    "v_accvgpr_read_b32 v83, a11\n"
    "v_accvgpr_read_b32 v84, a40\n"
    "v_accvgpr_read_b32 v85, a41\n"
    "v_accvgpr_read_b32 v86, a42\n"
    "v_accvgpr_read_b32 v87, a43\n"
    "v_accvgpr_read_b32 v88, a72\n"
    "v_accvgpr_read_b32 v89, a73\n"
    "v_accvgpr_read_b32 v90, a74\n"
    "v_accvgpr_read_b32 v91, a75\n"
    "v_accvgpr_read_b32 v92, a104\n"
    "v_accvgpr_read_b32 v93, a105\n"
    "v_accvgpr_read_b32 v94, a106\n"
    "v_accvgpr_read_b32 v95, a107\n"
    "v_accvgpr_read_b32 v96, a136\n"
    "v_accvgpr_read_b32 v97, a137\n"
    "v_accvgpr_read_b32 v98, a138\n"
    "v_accvgpr_read_b32 v99, a139\n"
    "v_cvt_pk_bf16_f32 v152, v80, v81\n"
    "v_cvt_pk_bf16_f32 v153, v82, v83\n"
    "v_cvt_pk_bf16_f32 v154, v84, v85\n"
    "v_cvt_pk_bf16_f32 v155, v86, v87\n"
    "v_cvt_pk_bf16_f32 v156, v88, v89\n"
    "v_cvt_pk_bf16_f32 v157, v90, v91\n"
    "v_cvt_pk_bf16_f32 v158, v92, v93\n"
    "v_cvt_pk_bf16_f32 v159, v94, v95\n"
    "v_cvt_pk_bf16_f32 v160, v96, v97\n"
    "v_cvt_pk_bf16_f32 v161, v98, v99\n"
    "s_add_u32 s65, s64, 5120\n"
    "buffer_store_dwordx4 v[152:155], v234, s[48:51], s65 offen sc1\n"
    "buffer_store_dwordx4 v[156:159], v234, s[48:51], s65 offen offset:1024 sc1\n"
    "buffer_store_dwordx2 v[160:161], v235, s[48:51], s65 offen offset:2048 sc1\n"
    "v_accvgpr_read_b32 v104, a12\n"
    "v_accvgpr_read_b32 v105, a13\n"
    "v_accvgpr_read_b32 v106, a14\n"
    "v_accvgpr_read_b32 v107, a15\n"
    "v_accvgpr_read_b32 v108, a44\n"
    "v_accvgpr_read_b32 v109, a45\n"
    "v_accvgpr_read_b32 v110, a46\n"
    "v_accvgpr_read_b32 v111, a47\n"
    "v_accvgpr_read_b32 v112, a76\n"
    "v_accvgpr_read_b32 v113, a77\n"
    "v_accvgpr_read_b32 v114, a78\n"
    "v_accvgpr_read_b32 v115, a79\n"
    "v_accvgpr_read_b32 v116, a108\n"
    "v_accvgpr_read_b32 v117, a109\n"
    "v_accvgpr_read_b32 v118, a110\n"
    "v_accvgpr_read_b32 v119, a111\n"
    "v_accvgpr_read_b32 v120, a140\n"
    "v_accvgpr_read_b32 v121, a141\n"
    "v_accvgpr_read_b32 v122, a142\n"
    "v_accvgpr_read_b32 v123, a143\n"
    "v_cvt_pk_bf16_f32 v164, v104, v105\n"
    "v_cvt_pk_bf16_f32 v165, v106, v107\n"
    "v_cvt_pk_bf16_f32 v166, v108, v109\n"
    "v_cvt_pk_bf16_f32 v167, v110, v111\n"
    "v_cvt_pk_bf16_f32 v168, v112, v113\n"
    "v_cvt_pk_bf16_f32 v169, v114, v115\n"
    "v_cvt_pk_bf16_f32 v170, v116, v117\n"
    "v_cvt_pk_bf16_f32 v171, v118, v119\n"
    "v_cvt_pk_bf16_f32 v172, v120, v121\n"
    "v_cvt_pk_bf16_f32 v173, v122, v123\n"
    "s_add_u32 s65, s64, 7680\n"
    "buffer_store_dwordx4 v[164:167], v234, s[48:51], s65 offen sc1\n"
    "buffer_store_dwordx4 v[168:171], v234, s[48:51], s65 offen offset:1024 sc1\n"
    "buffer_store_dwordx2 v[172:173], v235, s[48:51], s65 offen offset:2048 sc1\n"
    "v_accvgpr_read_b32 v32, a16\n"
    "v_accvgpr_read_b32 v33, a17\n"
    "v_accvgpr_read_b32 v34, a18\n"
    "v_accvgpr_read_b32 v35, a19\n"
    "v_accvgpr_read_b32 v36, a48\n"
    "v_accvgpr_read_b32 v37, a49\n"
    "v_accvgpr_read_b32 v38, a50\n"
    "v_accvgpr_read_b32 v39, a51\n"
    "v_accvgpr_read_b32 v40, a80\n"
    "v_accvgpr_read_b32 v41, a81\n"
    "v_accvgpr_read_b32 v42, a82\n"
    "v_accvgpr_read_b32 v43, a83\n"
    "v_accvgpr_read_b32 v44, a112\n"
    "v_accvgpr_read_b32 v45, a113\n"
    "v_accvgpr_read_b32 v46, a114\n"
    "v_accvgpr_read_b32 v47, a115\n"
    "v_accvgpr_read_b32 v48, a144\n"
    "v_accvgpr_read_b32 v49, a145\n"
    "v_accvgpr_read_b32 v50, a146\n"
    "v_accvgpr_read_b32 v51, a147\n"
    "v_cvt_pk_bf16_f32 v128, v32, v33\n"
    "v_cvt_pk_bf16_f32 v129, v34, v35\n"
    "v_cvt_pk_bf16_f32 v130, v36, v37\n"
    "v_cvt_pk_bf16_f32 v131, v38, v39\n"
    "v_cvt_pk_bf16_f32 v132, v40, v41\n"
    "v_cvt_pk_bf16_f32 v133, v42, v43\n"
    "v_cvt_pk_bf16_f32 v134, v44, v45\n"
    "v_cvt_pk_bf16_f32 v135, v46, v47\n"
    "v_cvt_pk_bf16_f32 v136, v48, v49\n"
    "v_cvt_pk_bf16_f32 v137, v50, v51\n"
    "s_add_u32 s65, s64, 10240\n"
    "buffer_store_dwordx4 v[128:131], v234, s[48:51], s65 offen sc1\n"
    "buffer_store_dwordx4 v[132:135], v234, s[48:51], s65 offen offset:1024 sc1\n"
    "buffer_store_dwordx2 v[136:137], v235, s[48:51], s65 offen offset:2048 sc1\n"
    "v_accvgpr_read_b32 v56, a20\n"
    "v_accvgpr_read_b32 v57, a21\n"
    "v_accvgpr_read_b32 v58, a22\n"
    "v_accvgpr_read_b32 v59, a23\n"
    "v_accvgpr_read_b32 v60, a52\n"
    "v_accvgpr_read_b32 v61, a53\n"
    "v_accvgpr_read_b32 v62, a54\n"
    "v_accvgpr_read_b32 v63, a55\n"
    "v_accvgpr_read_b32 v64, a84\n"
    "v_accvgpr_read_b32 v65, a85\n"
    "v_accvgpr_read_b32 v66, a86\n"
    "v_accvgpr_read_b32 v67, a87\n"
    "v_accvgpr_read_b32 v68, a116\n"
    "v_accvgpr_read_b32 v69, a117\n"
    "v_accvgpr_read_b32 v70, a118\n"
    "v_accvgpr_read_b32 v71, a119\n"
    "v_accvgpr_read_b32 v72, a148\n"
    "v_accvgpr_read_b32 v73, a149\n"
    "v_accvgpr_read_b32 v74, a150\n"
    "v_accvgpr_read_b32 v75, a151\n"
    "v_cvt_pk_bf16_f32 v140, v56, v57\n"
    "v_cvt_pk_bf16_f32 v141, v58, v59\n"
    "v_cvt_pk_bf16_f32 v142, v60, v61\n"
    "v_cvt_pk_bf16_f32 v143, v62, v63\n"
    "v_cvt_pk_bf16_f32 v144, v64, v65\n"
    "v_cvt_pk_bf16_f32 v145, v66, v67\n"
    "v_cvt_pk_bf16_f32 v146, v68, v69\n"
    "v_cvt_pk_bf16_f32 v147, v70, v71\n"
    "v_cvt_pk_bf16_f32 v148, v72, v73\n"
    "v_cvt_pk_bf16_f32 v149, v74, v75\n"
    "s_add_u32 s65, s64, 12800\n"
    "buffer_store_dwordx4 v[140:143], v234, s[48:51], s65 offen sc1\n"
    "buffer_store_dwordx4 v[144:147], v234, s[48:51], s65 offen offset:1024 sc1\n"
    "buffer_store_dwordx2 v[148:149], v235, s[48:51], s65 offen offset:2048 sc1\n"
    "v_accvgpr_read_b32 v80, a24\n"
    "v_accvgpr_read_b32 v81, a25\n"
    "v_accvgpr_read_b32 v82, a26\n"
    "v_accvgpr_read_b32 v83, a27\n"
    "v_accvgpr_read_b32 v84, a56\n"
    "v_accvgpr_read_b32 v85, a57\n"
    "v_accvgpr_read_b32 v86, a58\n"
    "v_accvgpr_read_b32 v87, a59\n"
    "v_accvgpr_read_b32 v88, a88\n"
    "v_accvgpr_read_b32 v89, a89\n"
    "v_accvgpr_read_b32 v90, a90\n"
    "v_accvgpr_read_b32 v91, a91\n"
    "v_accvgpr_read_b32 v92, a120\n"
    "v_accvgpr_read_b32 v93, a121\n"
    "v_accvgpr_read_b32 v94, a122\n"
    "v_accvgpr_read_b32 v95, a123\n"
    "v_accvgpr_read_b32 v96, a152\n"
    "v_accvgpr_read_b32 v97, a153\n"
    "v_accvgpr_read_b32 v98, a154\n"
    "v_accvgpr_read_b32 v99, a155\n"
    "v_cvt_pk_bf16_f32 v152, v80, v81\n"
    "v_cvt_pk_bf16_f32 v153, v82, v83\n"
    "v_cvt_pk_bf16_f32 v154, v84, v85\n"
    "v_cvt_pk_bf16_f32 v155, v86, v87\n"
    "v_cvt_pk_bf16_f32 v156, v88, v89\n"
    "v_cvt_pk_bf16_f32 v157, v90, v91\n"
    "v_cvt_pk_bf16_f32 v158, v92, v93\n"
    "v_cvt_pk_bf16_f32 v159, v94, v95\n"
    "v_cvt_pk_bf16_f32 v160, v96, v97\n"
    "v_cvt_pk_bf16_f32 v161, v98, v99\n"
    "s_add_u32 s65, s64, 15360\n"
    "buffer_store_dwordx4 v[152:155], v234, s[48:51], s65 offen sc1\n"
    "buffer_store_dwordx4 v[156:159], v234, s[48:51], s65 offen offset:1024 sc1\n"
    "buffer_store_dwordx2 v[160:161], v235, s[48:51], s65 offen offset:2048 sc1\n"
    "v_accvgpr_read_b32 v104, a28\n"
    "v_accvgpr_read_b32 v105, a29\n"
    "v_accvgpr_read_b32 v106, a30\n"
    "v_accvgpr_read_b32 v107, a31\n"
    "v_accvgpr_read_b32 v108, a60\n"
    "v_accvgpr_read_b32 v109, a61\n"
    "v_accvgpr_read_b32 v110, a62\n"
    "v_accvgpr_read_b32 v111, a63\n"
    "v_accvgpr_read_b32 v112, a92\n"
    "v_accvgpr_read_b32 v113, a93\n"
    "v_accvgpr_read_b32 v114, a94\n"
    "v_accvgpr_read_b32 v115, a95\n"
    "v_accvgpr_read_b32 v116, a124\n"
    "v_accvgpr_read_b32 v117, a125\n"
    "v_accvgpr_read_b32 v118, a126\n"
    "v_accvgpr_read_b32 v119, a127\n"
    "v_accvgpr_read_b32 v120, a156\n"
    "v_accvgpr_read_b32 v121, a157\n"
    "v_accvgpr_read_b32 v122, a158\n"
    "v_accvgpr_read_b32 v123, a159\n"
    "v_cvt_pk_bf16_f32 v164, v104, v105\n"
    "v_cvt_pk_bf16_f32 v165, v106, v107\n"
    "v_cvt_pk_bf16_f32 v166, v108, v109\n"
    "v_cvt_pk_bf16_f32 v167, v110, v111\n"
    "v_cvt_pk_bf16_f32 v168, v112, v113\n"
    "v_cvt_pk_bf16_f32 v169, v114, v115\n"
    "v_cvt_pk_bf16_f32 v170, v116, v117\n"
    "v_cvt_pk_bf16_f32 v171, v118, v119\n"
    "v_cvt_pk_bf16_f32 v172, v120, v121\n"
    "v_cvt_pk_bf16_f32 v173, v122, v123\n"
    "s_add_u32 s65, s64, 17920\n"
    "buffer_store_dwordx4 v[164:167], v234, s[48:51], s65 offen sc1\n"
    "buffer_store_dwordx4 v[168:171], v234, s[48:51], s65 offen offset:1024 sc1\n"
    "buffer_store_dwordx2 v[172:173], v235, s[48:51], s65 offen offset:2048 sc1\n"
    "s_waitcnt vmcnt(0)\n"
    "s_barrier\n"
    "s_cmp_lg_u32 s58, 0\n"
    "s_cbranch_scc1 D2S_SKIP\n"
    "s_mov_b64 exec, 1\n"
    "s_lshl_b32 s69, s61, 8\n"
    "s_add_u32 s69, s69, 196608\n"
    "v_mov_b32 v250, s69\n"
    "v_mov_b32 v251, s80\n"
    "global_store_dword v250, v251, s[56:57] sc1\n"
    "s_mov_b64 exec, -1\n"
    "D2S_SKIP:\n"
    "s_cmp_lg_u32 s58, 0\n"
    "s_cbranch_scc1 D2W_SKIP\n"
    "s_mov_b64 exec, 0xfe\n"
    "s_nop 0\n"
    "v_add_u32 v250, 196608, v240\n"
    "s_mov_b32 s82, 0\n"
    "D2W_LOOP:\n"
    "global_load_dword v251, v250, s[56:57] sc1\n"
    "s_waitcnt vmcnt(0)\n"
    "v_cmp_ne_u32 vcc, v251, v239\n"
    "s_add_u32 s82, s82, 1\n"
    "s_cmp_gt_u32 s82, 0x20000\n"
    "s_cbranch_scc1 D2W_END\n"
    "s_nop 2\n"
    "s_cbranch_vccz D2W_END\n"
    "s_sleep 1\n"
    "s_branch D2W_LOOP\n"
    "D2W_END:\n"
    "s_mov_b64 exec, -1\n"
    "D2W_SKIP:\n"
    "s_barrier\n"
    "s_lshr_b32 s64, s62, 1\n"
    "s_mul_i32 s64, s64, 20480\n"
    "s_and_b32 s65, s62, 1\n"
    "s_lshl_b32 s65, s65, 2\n"
    "s_add_u32 s65, s65, s58\n"
    "s_mul_i32 s66, s65, 2560\n"
    "s_add_u32 s64, s64, s66\n"
    "s_add_u32 s66, s64, 0\n"
    "buffer_load_dwordx4 v[32:35], v234, s[48:51], s66 offen sc1\n"
    "buffer_load_dwordx4 v[36:39], v234, s[48:51], s66 offen offset:1024 sc1\n"
    "buffer_load_dwordx2 v[40:41], v235, s[48:51], s66 offen offset:2048 sc1\n"
    "s_add_u32 s66, s64, 81920\n"
    "buffer_load_dwordx4 v[44:47], v234, s[48:51], s66 offen sc1\n"
    "buffer_load_dwordx4 v[48:51], v234, s[48:51], s66 offen offset:1024 sc1\n"
    "buffer_load_dwordx2 v[52:53], v235, s[48:51], s66 offen offset:2048 sc1\n"
    "s_add_u32 s66, s64, 163840\n"
    "buffer_load_dwordx4 v[56:59], v234, s[48:51], s66 offen sc1\n"
    "buffer_load_dwordx4 v[60:63], v234, s[48:51], s66 offen offset:1024 sc1\n"
    "buffer_load_dwordx2 v[64:65], v235, s[48:51], s66 offen offset:2048 sc1\n"
    "s_add_u32 s66, s64, 245760\n"
    "buffer_load_dwordx4 v[68:71], v234, s[48:51], s66 offen sc1\n"
    "buffer_load_dwordx4 v[72:75], v234, s[48:51], s66 offen offset:1024 sc1\n"
    "buffer_load_dwordx2 v[76:77], v235, s[48:51], s66 offen offset:2048 sc1\n"
    "s_add_u32 s66, s64, 327680\n"
    "buffer_load_dwordx4 v[80:83], v234, s[48:51], s66 offen sc1\n"
    "buffer_load_dwordx4 v[84:87], v234, s[48:51], s66 offen offset:1024 sc1\n"
    "buffer_load_dwordx2 v[88:89], v235, s[48:51], s66 offen offset:2048 sc1\n"
    "s_add_u32 s66, s64, 409600\n"
    "buffer_load_dwordx4 v[92:95], v234, s[48:51], s66 offen sc1\n"
    "buffer_load_dwordx4 v[96:99], v234, s[48:51], s66 offen offset:1024 sc1\n"
    "buffer_load_dwordx2 v[100:101], v235, s[48:51], s66 offen offset:2048 sc1\n"
    "s_add_u32 s66, s64, 491520\n"
    "buffer_load_dwordx4 v[104:107], v234, s[48:51], s66 offen sc1\n"
    "buffer_load_dwordx4 v[108:111], v234, s[48:51], s66 offen offset:1024 sc1\n"
    "buffer_load_dwordx2 v[112:113], v235, s[48:51], s66 offen offset:2048 sc1\n"
    "s_add_u32 s66, s64, 573440\n"
    "buffer_load_dwordx4 v[116:119], v234, s[48:51], s66 offen sc1\n"
    "buffer_load_dwordx4 v[120:123], v234, s[48:51], s66 offen offset:1024 sc1\n"
    "buffer_load_dwordx2 v[124:125], v235, s[48:51], s66 offen offset:2048 sc1\n"
    "s_lshr_b32 s66, s62, 1\n"
    "s_and_b32 s67, s66, 1\n"
    "s_mul_i32 s67, s67, 163840\n"
    "s_lshr_b32 s66, s66, 1\n"
    "s_lshl_b32 s66, s66, 8\n"
    "s_add_u32 s67, s67, s66\n"
    "s_lshl_b32 s65, s65, 5\n"
    "s_add_u32 s67, s67, s65\n"
    "v_and_b32 v244, 15, v1\n"
    "v_lshlrev_b32 v244, 11, v244\n"
    "v_lshrrev_b32 v245, 4, v1\n"
    "v_lshl_add_u32 v244, v245, 3, v244\n"
    "s_waitcnt vmcnt(23)\n"
    "v_lshlrev_b32 v144, 16, v32\n"
    "v_and_b32 v145, 0xffff0000, v32\n"
    "v_lshlrev_b32 v146, 16, v33\n"
    "v_and_b32 v147, 0xffff0000, v33\n"
    "v_lshlrev_b32 v148, 16, v34\n"
    "v_and_b32 v149, 0xffff0000, v34\n"
    "v_lshlrev_b32 v150, 16, v35\n"
    "v_and_b32 v151, 0xffff0000, v35\n"
    "s_waitcnt vmcnt(22)\n"
    "v_lshlrev_b32 v152, 16, v36\n"
    "v_and_b32 v153, 0xffff0000, v36\n"
    "v_lshlrev_b32 v154, 16, v37\n"
    "v_and_b32 v155, 0xffff0000, v37\n"
    "v_lshlrev_b32 v156, 16, v38\n"
    "v_and_b32 v157, 0xffff0000, v38\n"
    "v_lshlrev_b32 v158, 16, v39\n"
    "v_and_b32 v159, 0xffff0000, v39\n"
    "s_waitcnt vmcnt(21)\n"
    "v_lshlrev_b32 v160, 16, v40\n"
    "v_and_b32 v161, 0xffff0000, v40\n"
    "v_lshlrev_b32 v162, 16, v41\n"
    "v_and_b32 v163, 0xffff0000, v41\n"
    "s_waitcnt vmcnt(20)\n"
    "v_lshlrev_b32 v246, 16, v44\n"
    "v_and_b32 v44, 0xffff0000, v44\n"
    "v_add_f32 v144, v144, v246\n"
    "v_add_f32 v145, v145, v44\n"
    "v_lshlrev_b32 v247, 16, v45\n"
    "v_and_b32 v45, 0xffff0000, v45\n"
    "v_add_f32 v146, v146, v247\n"
    "v_add_f32 v147, v147, v45\n"
    "v_lshlrev_b32 v246, 16, v46\n"
    "v_and_b32 v46, 0xffff0000, v46\n"
    "v_add_f32 v148, v148, v246\n"
    "v_add_f32 v149, v149, v46\n"
    "v_lshlrev_b32 v247, 16, v47\n"
    "v_and_b32 v47, 0xffff0000, v47\n"
    "v_add_f32 v150, v150, v247\n"
    "v_add_f32 v151, v151, v47\n"
    "s_waitcnt vmcnt(19)\n"
    "v_lshlrev_b32 v246, 16, v48\n"
    "v_and_b32 v48, 0xffff0000, v48\n"
    "v_add_f32 v152, v152, v246\n"
    "v_add_f32 v153, v153, v48\n"
    "v_lshlrev_b32 v247, 16, v49\n"
    "v_and_b32 v49, 0xffff0000, v49\n"
    "v_add_f32 v154, v154, v247\n"
    "v_add_f32 v155, v155, v49\n"
    "v_lshlrev_b32 v246, 16, v50\n"
    "v_and_b32 v50, 0xffff0000, v50\n"
    "v_add_f32 v156, v156, v246\n"
    "v_add_f32 v157, v157, v50\n"
    "v_lshlrev_b32 v247, 16, v51\n"
    "v_and_b32 v51, 0xffff0000, v51\n"
    "v_add_f32 v158, v158, v247\n"
    "v_add_f32 v159, v159, v51\n"
    "s_waitcnt vmcnt(18)\n"
    "v_lshlrev_b32 v246, 16, v52\n"
    "v_and_b32 v52, 0xffff0000, v52\n"
    "v_add_f32 v160, v160, v246\n"
    "v_add_f32 v161, v161, v52\n"
    "v_lshlrev_b32 v247, 16, v53\n"
    "v_and_b32 v53, 0xffff0000, v53\n"
    "v_add_f32 v162, v162, v247\n"
    "v_add_f32 v163, v163, v53\n"
    "s_waitcnt vmcnt(17)\n"
    "v_lshlrev_b32 v246, 16, v56\n"
    "v_and_b32 v56, 0xffff0000, v56\n"
    "v_add_f32 v144, v144, v246\n"
    "v_add_f32 v145, v145, v56\n"
    "v_lshlrev_b32 v247, 16, v57\n"
    "v_and_b32 v57, 0xffff0000, v57\n"
    "v_add_f32 v146, v146, v247\n"
    "v_add_f32 v147, v147, v57\n"
    "v_lshlrev_b32 v246, 16, v58\n"
    "v_and_b32 v58, 0xffff0000, v58\n"
    "v_add_f32 v148, v148, v246\n"
    "v_add_f32 v149, v149, v58\n"
    "v_lshlrev_b32 v247, 16, v59\n"
    "v_and_b32 v59, 0xffff0000, v59\n"
    "v_add_f32 v150, v150, v247\n"
    "v_add_f32 v151, v151, v59\n"
    "s_waitcnt vmcnt(16)\n"
    "v_lshlrev_b32 v246, 16, v60\n"
    "v_and_b32 v60, 0xffff0000, v60\n"
    "v_add_f32 v152, v152, v246\n"
    "v_add_f32 v153, v153, v60\n"
    "v_lshlrev_b32 v247, 16, v61\n"
    "v_and_b32 v61, 0xffff0000, v61\n"
    "v_add_f32 v154, v154, v247\n"
    "v_add_f32 v155, v155, v61\n"
    "v_lshlrev_b32 v246, 16, v62\n"
    "v_and_b32 v62, 0xffff0000, v62\n"
    "v_add_f32 v156, v156, v246\n"
    "v_add_f32 v157, v157, v62\n"
    "v_lshlrev_b32 v247, 16, v63\n"
    "v_and_b32 v63, 0xffff0000, v63\n"
    "v_add_f32 v158, v158, v247\n"
    "v_add_f32 v159, v159, v63\n"
    "s_waitcnt vmcnt(15)\n"
    "v_lshlrev_b32 v246, 16, v64\n"
    "v_and_b32 v64, 0xffff0000, v64\n"
    "v_add_f32 v160, v160, v246\n"
    "v_add_f32 v161, v161, v64\n"
    "v_lshlrev_b32 v247, 16, v65\n"
    "v_and_b32 v65, 0xffff0000, v65\n"
    "v_add_f32 v162, v162, v247\n"
    "v_add_f32 v163, v163, v65\n"
    "s_waitcnt vmcnt(14)\n"
    "v_lshlrev_b32 v246, 16, v68\n"
    "v_and_b32 v68, 0xffff0000, v68\n"
    "v_add_f32 v144, v144, v246\n"
    "v_add_f32 v145, v145, v68\n"
    "v_lshlrev_b32 v247, 16, v69\n"
    "v_and_b32 v69, 0xffff0000, v69\n"
    "v_add_f32 v146, v146, v247\n"
    "v_add_f32 v147, v147, v69\n"
    "v_lshlrev_b32 v246, 16, v70\n"
    "v_and_b32 v70, 0xffff0000, v70\n"
    "v_add_f32 v148, v148, v246\n"
    "v_add_f32 v149, v149, v70\n"
    "v_lshlrev_b32 v247, 16, v71\n"
    "v_and_b32 v71, 0xffff0000, v71\n"
    "v_add_f32 v150, v150, v247\n"
    "v_add_f32 v151, v151, v71\n"
    "s_waitcnt vmcnt(13)\n"
    "v_lshlrev_b32 v246, 16, v72\n"
    "v_and_b32 v72, 0xffff0000, v72\n"
    "v_add_f32 v152, v152, v246\n"
    "v_add_f32 v153, v153, v72\n"
    "v_lshlrev_b32 v247, 16, v73\n"
    "v_and_b32 v73, 0xffff0000, v73\n"
    "v_add_f32 v154, v154, v247\n"
    "v_add_f32 v155, v155, v73\n"
    "v_lshlrev_b32 v246, 16, v74\n"
    "v_and_b32 v74, 0xffff0000, v74\n"
    "v_add_f32 v156, v156, v246\n"
    "v_add_f32 v157, v157, v74\n"
    "v_lshlrev_b32 v247, 16, v75\n"
    "v_and_b32 v75, 0xffff0000, v75\n"
    "v_add_f32 v158, v158, v247\n"
    "v_add_f32 v159, v159, v75\n"
    "s_waitcnt vmcnt(12)\n"
    "v_lshlrev_b32 v246, 16, v76\n"
    "v_and_b32 v76, 0xffff0000, v76\n"
    "v_add_f32 v160, v160, v246\n"
    "v_add_f32 v161, v161, v76\n"
    "v_lshlrev_b32 v247, 16, v77\n"
    "v_and_b32 v77, 0xffff0000, v77\n"
    "v_add_f32 v162, v162, v247\n"
    "v_add_f32 v163, v163, v77\n"
    "s_waitcnt vmcnt(11)\n"
    "v_lshlrev_b32 v246, 16, v80\n"
    "v_and_b32 v80, 0xffff0000, v80\n"
    "v_add_f32 v144, v144, v246\n"
    "v_add_f32 v145, v145, v80\n"
    "v_lshlrev_b32 v247, 16, v81\n"
    "v_and_b32 v81, 0xffff0000, v81\n"
    "v_add_f32 v146, v146, v247\n"
    "v_add_f32 v147, v147, v81\n"
    "v_lshlrev_b32 v246, 16, v82\n"
    "v_and_b32 v82, 0xffff0000, v82\n"
    "v_add_f32 v148, v148, v246\n"
    "v_add_f32 v149, v149, v82\n"
    "v_lshlrev_b32 v247, 16, v83\n"
    "v_and_b32 v83, 0xffff0000, v83\n"
    "v_add_f32 v150, v150, v247\n"
    "v_add_f32 v151, v151, v83\n"
    "s_waitcnt vmcnt(10)\n"
    "v_lshlrev_b32 v246, 16, v84\n"
    "v_and_b32 v84, 0xffff0000, v84\n"
    "v_add_f32 v152, v152, v246\n"
    "v_add_f32 v153, v153, v84\n"
    "v_lshlrev_b32 v247, 16, v85\n"
    "v_and_b32 v85, 0xffff0000, v85\n"
    "v_add_f32 v154, v154, v247\n"
    "v_add_f32 v155, v155, v85\n"
    "v_lshlrev_b32 v246, 16, v86\n"
    "v_and_b32 v86, 0xffff0000, v86\n"
    "v_add_f32 v156, v156, v246\n"
    "v_add_f32 v157, v157, v86\n"
    "v_lshlrev_b32 v247, 16, v87\n"
    "v_and_b32 v87, 0xffff0000, v87\n"
    "v_add_f32 v158, v158, v247\n"
    "v_add_f32 v159, v159, v87\n"
    "s_waitcnt vmcnt(9)\n"
    "v_lshlrev_b32 v246, 16, v88\n"
    "v_and_b32 v88, 0xffff0000, v88\n"
    "v_add_f32 v160, v160, v246\n"
    "v_add_f32 v161, v161, v88\n"
    "v_lshlrev_b32 v247, 16, v89\n"
    "v_and_b32 v89, 0xffff0000, v89\n"
    "v_add_f32 v162, v162, v247\n"
    "v_add_f32 v163, v163, v89\n"
    "s_waitcnt vmcnt(8)\n"
    "v_lshlrev_b32 v246, 16, v92\n"
    "v_and_b32 v92, 0xffff0000, v92\n"
    "v_add_f32 v144, v144, v246\n"
    "v_add_f32 v145, v145, v92\n"
    "v_lshlrev_b32 v247, 16, v93\n"
    "v_and_b32 v93, 0xffff0000, v93\n"
    "v_add_f32 v146, v146, v247\n"
    "v_add_f32 v147, v147, v93\n"
    "v_lshlrev_b32 v246, 16, v94\n"
    "v_and_b32 v94, 0xffff0000, v94\n"
    "v_add_f32 v148, v148, v246\n"
    "v_add_f32 v149, v149, v94\n"
    "v_lshlrev_b32 v247, 16, v95\n"
    "v_and_b32 v95, 0xffff0000, v95\n"
    "v_add_f32 v150, v150, v247\n"
    "v_add_f32 v151, v151, v95\n"
    "s_waitcnt vmcnt(7)\n"
    "v_lshlrev_b32 v246, 16, v96\n"
    "v_and_b32 v96, 0xffff0000, v96\n"
    "v_add_f32 v152, v152, v246\n"
    "v_add_f32 v153, v153, v96\n"
    "v_lshlrev_b32 v247, 16, v97\n"
    "v_and_b32 v97, 0xffff0000, v97\n"
    "v_add_f32 v154, v154, v247\n"
    "v_add_f32 v155, v155, v97\n"
    "v_lshlrev_b32 v246, 16, v98\n"
    "v_and_b32 v98, 0xffff0000, v98\n"
    "v_add_f32 v156, v156, v246\n"
    "v_add_f32 v157, v157, v98\n"
    "v_lshlrev_b32 v247, 16, v99\n"
    "v_and_b32 v99, 0xffff0000, v99\n"
    "v_add_f32 v158, v158, v247\n"
    "v_add_f32 v159, v159, v99\n"
    "s_waitcnt vmcnt(6)\n"
    "v_lshlrev_b32 v246, 16, v100\n"
    "v_and_b32 v100, 0xffff0000, v100\n"
    "v_add_f32 v160, v160, v246\n"
    "v_add_f32 v161, v161, v100\n"
    "v_lshlrev_b32 v247, 16, v101\n"
    "v_and_b32 v101, 0xffff0000, v101\n"
    "v_add_f32 v162, v162, v247\n"
    "v_add_f32 v163, v163, v101\n"
    "s_waitcnt vmcnt(5)\n"
    "v_lshlrev_b32 v246, 16, v104\n"
    "v_and_b32 v104, 0xffff0000, v104\n"
    "v_add_f32 v144, v144, v246\n"
    "v_add_f32 v145, v145, v104\n"
    "v_lshlrev_b32 v247, 16, v105\n"
    "v_and_b32 v105, 0xffff0000, v105\n"
    "v_add_f32 v146, v146, v247\n"
    "v_add_f32 v147, v147, v105\n"
    "v_lshlrev_b32 v246, 16, v106\n"
    "v_and_b32 v106, 0xffff0000, v106\n"
    "v_add_f32 v148, v148, v246\n"
    "v_add_f32 v149, v149, v106\n"
    "v_lshlrev_b32 v247, 16, v107\n"
    "v_and_b32 v107, 0xffff0000, v107\n"
    "v_add_f32 v150, v150, v247\n"
    "v_add_f32 v151, v151, v107\n"
    "s_waitcnt vmcnt(4)\n"
    "v_lshlrev_b32 v246, 16, v108\n"
    "v_and_b32 v108, 0xffff0000, v108\n"
    "v_add_f32 v152, v152, v246\n"
    "v_add_f32 v153, v153, v108\n"
    "v_lshlrev_b32 v247, 16, v109\n"
    "v_and_b32 v109, 0xffff0000, v109\n"
    "v_add_f32 v154, v154, v247\n"
    "v_add_f32 v155, v155, v109\n"
    "v_lshlrev_b32 v246, 16, v110\n"
    "v_and_b32 v110, 0xffff0000, v110\n"
    "v_add_f32 v156, v156, v246\n"
    "v_add_f32 v157, v157, v110\n"
    "v_lshlrev_b32 v247, 16, v111\n"
    "v_and_b32 v111, 0xffff0000, v111\n"
    "v_add_f32 v158, v158, v247\n"
    "v_add_f32 v159, v159, v111\n"
    "s_waitcnt vmcnt(3)\n"
    "v_lshlrev_b32 v246, 16, v112\n"
    "v_and_b32 v112, 0xffff0000, v112\n"
    "v_add_f32 v160, v160, v246\n"
    "v_add_f32 v161, v161, v112\n"
    "v_lshlrev_b32 v247, 16, v113\n"
    "v_and_b32 v113, 0xffff0000, v113\n"
    "v_add_f32 v162, v162, v247\n"
    "v_add_f32 v163, v163, v113\n"
    "s_waitcnt vmcnt(2)\n"
    "v_lshlrev_b32 v246, 16, v116\n"
    "v_and_b32 v116, 0xffff0000, v116\n"
    "v_add_f32 v144, v144, v246\n"
    "v_add_f32 v145, v145, v116\n"
    "v_lshlrev_b32 v247, 16, v117\n"
    "v_and_b32 v117, 0xffff0000, v117\n"
    "v_add_f32 v146, v146, v247\n"
    "v_add_f32 v147, v147, v117\n"
    "v_lshlrev_b32 v246, 16, v118\n"
    "v_and_b32 v118, 0xffff0000, v118\n"
    "v_add_f32 v148, v148, v246\n"
    "v_add_f32 v149, v149, v118\n"
    "v_lshlrev_b32 v247, 16, v119\n"
    "v_and_b32 v119, 0xffff0000, v119\n"
    "v_add_f32 v150, v150, v247\n"
    "v_add_f32 v151, v151, v119\n"
    "s_waitcnt vmcnt(1)\n"
    "v_lshlrev_b32 v246, 16, v120\n"
    "v_and_b32 v120, 0xffff0000, v120\n"
    "v_add_f32 v152, v152, v246\n"
    "v_add_f32 v153, v153, v120\n"
    "v_lshlrev_b32 v247, 16, v121\n"
    "v_and_b32 v121, 0xffff0000, v121\n"
    "v_add_f32 v154, v154, v247\n"
    "v_add_f32 v155, v155, v121\n"
    "v_lshlrev_b32 v246, 16, v122\n"
    "v_and_b32 v122, 0xffff0000, v122\n"
    "v_add_f32 v156, v156, v246\n"
    "v_add_f32 v157, v157, v122\n"
    "v_lshlrev_b32 v247, 16, v123\n"
    "v_and_b32 v123, 0xffff0000, v123\n"
    "v_add_f32 v158, v158, v247\n"
    "v_add_f32 v159, v159, v123\n"
    "s_waitcnt vmcnt(0)\n"
    "v_lshlrev_b32 v246, 16, v124\n"
    "v_and_b32 v124, 0xffff0000, v124\n"
    "v_add_f32 v160, v160, v246\n"
    "v_add_f32 v161, v161, v124\n"
    "v_lshlrev_b32 v247, 16, v125\n"
    "v_and_b32 v125, 0xffff0000, v125\n"
    "v_add_f32 v162, v162, v247\n"
    "v_add_f32 v163, v163, v125\n"
    "v_cvt_pk_bf16_f32 v176, v144, v145\n"
    "v_cvt_pk_bf16_f32 v177, v146, v147\n"
    "s_add_u32 s64, s67, 0\n"
    "buffer_store_dwordx2 v[176:177], v244, s[52:55], s64 offen\n"
    "v_cvt_pk_bf16_f32 v178, v148, v149\n"
    "v_cvt_pk_bf16_f32 v179, v150, v151\n"
    "s_add_u32 s64, s67, 32768\n"
    "buffer_store_dwordx2 v[178:179], v244, s[52:55], s64 offen\n"
    "v_cvt_pk_bf16_f32 v180, v152, v153\n"
    "v_cvt_pk_bf16_f32 v181, v154, v155\n"
    "s_add_u32 s64, s67, 65536\n"
    "buffer_store_dwordx2 v[180:181], v244, s[52:55], s64 offen\n"
    "v_cvt_pk_bf16_f32 v182, v156, v157\n"
    "v_cvt_pk_bf16_f32 v183, v158, v159\n"
    "s_add_u32 s64, s67, 98304\n"
    "buffer_store_dwordx2 v[182:183], v244, s[52:55], s64 offen\n"
    "v_cvt_pk_bf16_f32 v184, v160, v161\n"
    "v_cvt_pk_bf16_f32 v185, v162, v163\n"
    "s_add_u32 s64, s67, 131072\n"
    "buffer_store_dwordx2 v[184:185], v244, s[52:55], s64 offen\n"
    "s_endpgm\n"
    "FALLBACK:\n"
    "s_waitcnt vmcnt(0)\n"
    "s_waitcnt lgkmcnt(0)\n"
    "s_barrier\n"
    "s_mov_b32 s85, 1\n"
    "v_mov_b32 v2, 0xff800000\n"
    "v_mov_b32 v3, 0xff800000\n"
    "v_mov_b32 v4, 0xff800000\n"
    "v_mov_b32 v5, 0xff800000\n"
    "v_mov_b32 v6, 0xff800000\n"
    "v_and_b32 v21, 0xffff, v21\n"
    "v_and_b32 v22, 0xffff, v22\n"
    "v_and_b32 v23, 0xffff, v23\n"
    "v_and_b32 v24, 0xffff, v24\n"
    "v_and_b32 v25, 0xffff, v25\n"
    "v_and_b32 v26, 0xffff, v26\n"
    "s_mov_b32 s75, s77\n"
    "s_mov_b32 s79, 16\n"
    "FB_LOOP:\n"
    "s_add_u32 m0, s78, 0\n"
    "s_nop 0\n"
    "buffer_load_dwordx4 v14, s[28:31], s75 offen offset:0 lds\n"
    "buffer_load_dwordx4 v15, s[28:31], s75 offen offset:1024 lds\n"
    "buffer_load_dwordx4 v16, s[28:31], s75 offen offset:2048 lds\n"
    "buffer_load_dwordx4 v17, s[28:31], s75 offen offset:3072 lds\n"
    "s_add_u32 s75, s75, 8192\n"
    "s_waitcnt vmcnt(0)\n"
    "s_barrier\n"
    "ds_read_b128 a[192:195], v21 offset:0\n"
    "ds_read_b128 a[196:199], v21 offset:4096\n"
    "ds_read_b128 a[200:203], v22 offset:0\n"
    "ds_read_b128 a[204:207], v22 offset:4096\n"
    "ds_read_b128 a[208:211], v23 offset:0\n"
    "ds_read_b128 a[212:215], v23 offset:4096\n"
    "ds_read_b128 a[216:219], v24 offset:0\n"
    "ds_read_b128 a[220:223], v24 offset:4096\n"
    "s_waitcnt lgkmcnt(0)\n"
    "s_barrier\n"
    "v_mfma_f32_16x16x32_bf16 v[112:115], a[192:195], v[32:35], 0\n"
    "v_mfma_f32_16x16x32_bf16 v[116:119], a[196:199], v[32:35], 0\n"
    "v_mfma_f32_16x16x32_bf16 v[112:115], a[200:203], v[36:39], v[112:115]\n"
    "v_mfma_f32_16x16x32_bf16 v[116:119], a[204:207], v[36:39], v[116:119]\n"
    "v_mfma_f32_16x16x32_bf16 v[112:115], a[208:211], v[40:43], v[112:115]\n"
    "v_mfma_f32_16x16x32_bf16 v[116:119], a[212:215], v[40:43], v[116:119]\n"
    "v_mfma_f32_16x16x32_bf16 v[112:115], a[216:219], v[44:47], v[112:115]\n"
    "v_mfma_f32_16x16x32_bf16 v[116:119], a[220:223], v[44:47], v[116:119]\n"
    "s_nop 7\n"
    "v_max3_f32 v2, v2, v112, v113\n"
    "v_max3_f32 v2, v2, v114, v115\n"
    "v_max3_f32 v2, v2, v116, v117\n"
    "v_max3_f32 v2, v2, v118, v119\n"
    "v_mfma_f32_16x16x32_bf16 v[120:123], a[192:195], v[48:51], 0\n"
    "v_mfma_f32_16x16x32_bf16 v[124:127], a[196:199], v[48:51], 0\n"
    "v_mfma_f32_16x16x32_bf16 v[120:123], a[200:203], v[52:55], v[120:123]\n"
    "v_mfma_f32_16x16x32_bf16 v[124:127], a[204:207], v[52:55], v[124:127]\n"
    "v_mfma_f32_16x16x32_bf16 v[120:123], a[208:211], v[56:59], v[120:123]\n"
    "v_mfma_f32_16x16x32_bf16 v[124:127], a[212:215], v[56:59], v[124:127]\n"
    "v_mfma_f32_16x16x32_bf16 v[120:123], a[216:219], v[60:63], v[120:123]\n"
    "v_mfma_f32_16x16x32_bf16 v[124:127], a[220:223], v[60:63], v[124:127]\n"
    "s_nop 7\n"
    "v_max3_f32 v3, v3, v120, v121\n"
    "v_max3_f32 v3, v3, v122, v123\n"
    "v_max3_f32 v3, v3, v124, v125\n"
    "v_max3_f32 v3, v3, v126, v127\n"
    "v_mfma_f32_16x16x32_bf16 v[128:131], a[192:195], v[64:67], 0\n"
    "v_mfma_f32_16x16x32_bf16 v[132:135], a[196:199], v[64:67], 0\n"
    "v_mfma_f32_16x16x32_bf16 v[128:131], a[200:203], v[68:71], v[128:131]\n"
    "v_mfma_f32_16x16x32_bf16 v[132:135], a[204:207], v[68:71], v[132:135]\n"
    "v_mfma_f32_16x16x32_bf16 v[128:131], a[208:211], v[72:75], v[128:131]\n"
    "v_mfma_f32_16x16x32_bf16 v[132:135], a[212:215], v[72:75], v[132:135]\n"
    "v_mfma_f32_16x16x32_bf16 v[128:131], a[216:219], v[76:79], v[128:131]\n"
    "v_mfma_f32_16x16x32_bf16 v[132:135], a[220:223], v[76:79], v[132:135]\n"
    "s_nop 7\n"
    "v_max3_f32 v4, v4, v128, v129\n"
    "v_max3_f32 v4, v4, v130, v131\n"
    "v_max3_f32 v4, v4, v132, v133\n"
    "v_max3_f32 v4, v4, v134, v135\n"
    "v_mfma_f32_16x16x32_bf16 v[136:139], a[192:195], v[80:83], 0\n"
    "v_mfma_f32_16x16x32_bf16 v[140:143], a[196:199], v[80:83], 0\n"
    "v_mfma_f32_16x16x32_bf16 v[136:139], a[200:203], v[84:87], v[136:139]\n"
    "v_mfma_f32_16x16x32_bf16 v[140:143], a[204:207], v[84:87], v[140:143]\n"
    "v_mfma_f32_16x16x32_bf16 v[136:139], a[208:211], v[88:91], v[136:139]\n"
    "v_mfma_f32_16x16x32_bf16 v[140:143], a[212:215], v[88:91], v[140:143]\n"
    "v_mfma_f32_16x16x32_bf16 v[136:139], a[216:219], v[92:95], v[136:139]\n"
    "v_mfma_f32_16x16x32_bf16 v[140:143], a[220:223], v[92:95], v[140:143]\n"
    "s_nop 7\n"
    "v_max3_f32 v5, v5, v136, v137\n"
    "v_max3_f32 v5, v5, v138, v139\n"
    "v_max3_f32 v5, v5, v140, v141\n"
    "v_max3_f32 v5, v5, v142, v143\n"
    "v_mfma_f32_16x16x32_bf16 v[144:147], a[192:195], v[96:99], 0\n"
    "v_mfma_f32_16x16x32_bf16 v[148:151], a[196:199], v[96:99], 0\n"
    "v_mfma_f32_16x16x32_bf16 v[144:147], a[200:203], v[100:103], v[144:147]\n"
    "v_mfma_f32_16x16x32_bf16 v[148:151], a[204:207], v[100:103], v[148:151]\n"
    "v_mfma_f32_16x16x32_bf16 v[144:147], a[208:211], v[104:107], v[144:147]\n"
    "v_mfma_f32_16x16x32_bf16 v[148:151], a[212:215], v[104:107], v[148:151]\n"
    "v_mfma_f32_16x16x32_bf16 v[144:147], a[216:219], v[108:111], v[144:147]\n"
    "v_mfma_f32_16x16x32_bf16 v[148:151], a[220:223], v[108:111], v[148:151]\n"
    "s_nop 7\n"
    "v_max3_f32 v6, v6, v144, v145\n"
    "v_max3_f32 v6, v6, v146, v147\n"
    "v_max3_f32 v6, v6, v148, v149\n"
    "v_max3_f32 v6, v6, v150, v151\n"
    "s_sub_u32 s79, s79, 1\n"
    "s_cmp_lg_u32 s79, 0\n"
    "s_cbranch_scc1 FB_LOOP\n"
    "s_mov_b32 s75, s77\n"
    "s_mov_b32 s76, 0\n"
    "s_add_u32 m0, s76, s78\n"
    "s_nop 0\n"
    "buffer_load_dwordx4 v14, s[28:31], s75 offen offset:0 nt lds\n"
    "buffer_load_dwordx4 v15, s[28:31], s75 offen offset:1024 nt lds\n"
    "buffer_load_dwordx4 v16, s[28:31], s75 offen offset:2048 nt lds\n"
    "buffer_load_dwordx4 v17, s[28:31], s75 offen offset:3072 nt lds\n"
    "s_add_u32 s64, s76, s78\n"
    "s_add_u32 m0, s64, 16384\n"
    "s_nop 0\n"
    "buffer_load_dwordx4 v18, s[36:39], s75 offen offset:0 nt lds\n"
    "buffer_load_dwordx4 v19, s[36:39], s75 offen offset:1024 nt lds\n"
    "buffer_load_dwordx4 v18, s[36:39], s75 offen offset:2048 nt lds\n"
    "buffer_load_dwordx4 v19, s[36:39], s75 offen offset:3072 nt lds\n"
    "s_add_u32 s75, s75, 8192\n"
    "s_add_u32 s76, s76, 32768\n"
    "s_and_b32 s76, s76, 0x1ffff\n"
    "s_add_u32 m0, s76, s78\n"
    "s_nop 0\n"
    "buffer_load_dwordx4 v14, s[28:31], s75 offen offset:0 nt lds\n"
    "buffer_load_dwordx4 v15, s[28:31], s75 offen offset:1024 nt lds\n"
    "buffer_load_dwordx4 v16, s[28:31], s75 offen offset:2048 nt lds\n"
    "buffer_load_dwordx4 v17, s[28:31], s75 offen offset:3072 nt lds\n"
    "s_add_u32 s64, s76, s78\n"
    "s_add_u32 m0, s64, 16384\n"
    "s_nop 0\n"
    "buffer_load_dwordx4 v18, s[36:39], s75 offen offset:0 nt lds\n"
    "buffer_load_dwordx4 v19, s[36:39], s75 offen offset:1024 nt lds\n"
    "buffer_load_dwordx4 v18, s[36:39], s75 offen offset:2048 nt lds\n"
    "buffer_load_dwordx4 v19, s[36:39], s75 offen offset:3072 nt lds\n"
    "s_add_u32 s75, s75, 8192\n"
    "s_add_u32 s76, s76, 32768\n"
    "s_and_b32 s76, s76, 0x1ffff\n"
    "s_add_u32 m0, s76, s78\n"
    "s_nop 0\n"
    "buffer_load_dwordx4 v14, s[28:31], s75 offen offset:0 nt lds\n"
    "buffer_load_dwordx4 v15, s[28:31], s75 offen offset:1024 nt lds\n"
    "buffer_load_dwordx4 v16, s[28:31], s75 offen offset:2048 nt lds\n"
    "buffer_load_dwordx4 v17, s[28:31], s75 offen offset:3072 nt lds\n"
    "s_add_u32 s64, s76, s78\n"
    "s_add_u32 m0, s64, 16384\n"
    "s_nop 0\n"
    "buffer_load_dwordx4 v18, s[36:39], s75 offen offset:0 nt lds\n"
    "buffer_load_dwordx4 v19, s[36:39], s75 offen offset:1024 nt lds\n"
    "buffer_load_dwordx4 v18, s[36:39], s75 offen offset:2048 nt lds\n"
    "buffer_load_dwordx4 v19, s[36:39], s75 offen offset:3072 nt lds\n"
    "s_add_u32 s75, s75, 8192\n"
    "s_add_u32 s76, s76, 32768\n"
    "s_and_b32 s76, s76, 0x1ffff\n"
    "s_waitcnt vmcnt(0)\n"
    "s_barrier\n"
    "ds_read_b128 a[192:195], v21 offset:0\n"
    "ds_read_b128 a[196:199], v21 offset:4096\n"
    "ds_read_b128 a[200:203], v22 offset:0\n"
    "ds_read_b128 a[204:207], v22 offset:4096\n"
    "ds_read_b128 a[208:211], v23 offset:0\n"
    "ds_read_b128 a[212:215], v23 offset:4096\n"
    "ds_read_b128 a[216:219], v24 offset:0\n"
    "ds_read_b128 a[220:223], v24 offset:4096\n"
    "ds_read_b64_tr_b16 a[224:225], v25 offset:0\n"
    "ds_read_b64_tr_b16 a[226:227], v25 offset:4096\n"
    "ds_read_b64_tr_b16 a[228:229], v26 offset:0\n"
    "ds_read_b64_tr_b16 a[230:231], v26 offset:4096\n"
    "ds_read_b64_tr_b16 a[232:233], v25 offset:512\n"
    "ds_read_b64_tr_b16 a[234:235], v25 offset:4608\n"
    "ds_read_b64_tr_b16 a[236:237], v26 offset:512\n"
    "ds_read_b64_tr_b16 a[238:239], v26 offset:4608\n"
    "ds_read_b64_tr_b16 a[240:241], v25 offset:1024\n"
    "ds_read_b64_tr_b16 a[242:243], v25 offset:5120\n"
    "ds_read_b64_tr_b16 a[244:245], v26 offset:1024\n"
    "ds_read_b64_tr_b16 a[246:247], v26 offset:5120\n"
    "ds_read_b64_tr_b16 a[248:249], v25 offset:1536\n"
    "ds_read_b64_tr_b16 a[250:251], v25 offset:5632\n"
    "ds_read_b64_tr_b16 a[252:253], v26 offset:1536\n"
    "ds_read_b64_tr_b16 a[254:255], v26 offset:5632\n"
    "v_add_u32 v232, 0xfffdc000, v232\n"
    "v_add_u32 v233, 0xfffdc000, v233\n"
    "s_waitcnt lgkmcnt(0)\n"
    "s_branch ATT_START\n"
      :
      : "s"(xg), "s"(wqg), "s"(kg), "s"(vg), "s"(wog), "s"(ws), "s"(outg), "s"(g), "v"(threadIdx.x), "s"(ldsb)
      : "v1", "v2", "v3", "v4", "v5", "v6", "v7", "v8", "v9", "v10", "v11", "v12", "v13", "v14", "v15", "v16",
        "v17", "v18", "v19", "v20", "v21", "v22", "v23", "v24", "v25", "v26", "v27", "v28", "v29", "v30", "v31", "v32",
        "v33", "v34", "v35", "v36", "v37", "v38", "v39", "v40", "v41", "v42", "v43", "v44", "v45", "v46", "v47", "v48",
        "v49", "v50", "v51", "v52", "v53", "v54", "v55", "v56", "v57", "v58", "v59", "v60", "v61", "v62", "v63", "v64",
        "v65", "v66", "v67", "v68", "v69", "v70", "v71", "v72", "v73", "v74", "v75", "v76", "v77", "v78", "v79", "v80",
        "v81", "v82", "v83", "v84", "v85", "v86", "v87", "v88", "v89", "v90", "v91", "v92", "v93", "v94", "v95", "v96",
        "v97", "v98", "v99", "v100", "v101", "v102", "v103", "v104", "v105", "v106", "v107", "v108", "v109", "v110", "v111", "v112",
        "v113", "v114", "v115", "v116", "v117", "v118", "v119", "v120", "v121", "v122", "v123", "v124", "v125", "v126", "v127", "v128",
        "v129", "v130", "v131", "v132", "v133", "v134", "v135", "v136", "v137", "v138", "v139", "v140", "v141", "v142", "v143", "v144",
        "v145", "v146", "v147", "v148", "v149", "v150", "v151", "v152", "v153", "v154", "v155", "v156", "v157", "v158", "v159", "v160",
        "v161", "v162", "v163", "v164", "v165", "v166", "v167", "v168", "v169", "v170", "v171", "v172", "v173", "v174", "v175", "v176",
        "v177", "v178", "v179", "v180", "v181", "v182", "v183", "v184", "v185", "v186", "v187", "v188", "v189", "v190", "v191", "v192",
        "v193", "v194", "v195", "v196", "v197", "v198", "v199", "v200", "v201", "v202", "v203", "v204", "v205", "v206", "v207", "v208",
        "v209", "v210", "v211", "v212", "v213", "v214", "v215", "v216", "v217", "v218", "v219", "v220", "v221", "v222", "v223", "v224",
        "v225", "v226", "v227", "v228", "v229", "v230", "v231", "v232", "v233", "v234", "v235", "v236", "v237", "v238", "v239", "v240",
        "v241", "v242", "v243", "v244", "v245", "v246", "v247", "v248", "v249", "v250", "v251", "v252", "v253", "v254", "v255", "a0",
        "a1", "a2", "a3", "a4", "a5", "a6", "a7", "a8", "a9", "a10", "a11", "a12", "a13", "a14", "a15", "a16",
        "a17", "a18", "a19", "a20", "a21", "a22", "a23", "a24", "a25", "a26", "a27", "a28", "a29", "a30", "a31", "a32",
        "a33", "a34", "a35", "a36", "a37", "a38", "a39", "a40", "a41", "a42", "a43", "a44", "a45", "a46", "a47", "a48",
        "a49", "a50", "a51", "a52", "a53", "a54", "a55", "a56", "a57", "a58", "a59", "a60", "a61", "a62", "a63", "a64",
        "a65", "a66", "a67", "a68", "a69", "a70", "a71", "a72", "a73", "a74", "a75", "a76", "a77", "a78", "a79", "a80",
        "a81", "a82", "a83", "a84", "a85", "a86", "a87", "a88", "a89", "a90", "a91", "a92", "a93", "a94", "a95", "a96",
        "a97", "a98", "a99", "a100", "a101", "a102", "a103", "a104", "a105", "a106", "a107", "a108", "a109", "a110", "a111", "a112",
        "a113", "a114", "a115", "a116", "a117", "a118", "a119", "a120", "a121", "a122", "a123", "a124", "a125", "a126", "a127", "a128",
        "a129", "a130", "a131", "a132", "a133", "a134", "a135", "a136", "a137", "a138", "a139", "a140", "a141", "a142", "a143", "a144",
        "a145", "a146", "a147", "a148", "a149", "a150", "a151", "a152", "a153", "a154", "a155", "a156", "a157", "a158", "a159", "a160",
        "a161", "a162", "a163", "a164", "a165", "a166", "a167", "a168", "a169", "a170", "a171", "a172", "a173", "a174", "a175", "a176",
        "a177", "a178", "a179", "a180", "a181", "a182", "a183", "a184", "a185", "a186", "a187", "a188", "a189", "a190", "a191", "a192",
        "a193", "a194", "a195", "a196", "a197", "a198", "a199", "a200", "a201", "a202", "a203", "a204", "a205", "a206", "a207", "a208",
        "a209", "a210", "a211", "a212", "a213", "a214", "a215", "a216", "a217", "a218", "a219", "a220", "a221", "a222", "a223", "a224",
        "a225", "a226", "a227", "a228", "a229", "a230", "a231", "a232", "a233", "a234", "a235", "a236", "a237", "a238", "a239", "a240",
        "a241", "a242", "a243", "a244", "a245", "a246", "a247", "a248", "a249", "a250", "a251", "a252", "a253", "a254", "a255", "s20",
        "s21", "s22", "s23", "s24", "s25", "s26", "s27", "s28", "s29", "s30", "s31", "s36", "s37", "s38", "s39", "s40",
        "s41", "s42", "s43", "s44", "s45", "s46", "s47", "s48", "s49", "s50", "s51", "s52", "s53", "s54", "s55", "s56",
        "s57", "s58", "s59", "s60", "s61", "s62", "s63", "s64", "s65", "s66", "s67", "s68", "s69", "s70", "s71", "s72",
        "s73", "s74", "s75", "s76", "s77", "s78", "s79", "s80", "s81", "s82", "s83", "s84", "s85", "s86", "s87", "s88",
        "s89", "s90", "s91", "s92", "s93", "s94", "s95", "s96", "s97", "s98", "s99", "vcc", "scc", "memory");
}

extern "C" void kernel_launch(void* const* d_in, const int* in_sizes, int n_in, void* d_out, int out_size, void* d_ws, size_t ws_size,
                              hipStream_t stream) {
  fused_attn_block<<<256, 256, 0, stream>>>((const u16*)d_in[0], (const u16*)d_in[1], (const u16*)d_in[2], (const u16*)d_in[3], (const u16*)d_in[4],
                                            (const u16*)d_in[5], (const u16*)d_in[6], (const u16*)d_in[7], (const u16*)d_in[8], (const u16*)d_in[9],
                                            (char*)d_ws, (u16*)d_out);
}
```
